```python
import torch

def _softmax(x, axis=-1):
    return torch.softmax(x, dim=axis)
def _gelu(x):
    return torch.nn.functional.gelu(x, approximate="tanh")
def _silu(x):
    return torch.nn.functional.silu(x)
def _sigmoid(x):
    return torch.sigmoid(x)
def _layernorm(x, axis=-1, eps=1e-5):
    m = x.mean(dim=axis, keepdim=True)
    v = x.var(dim=axis, keepdim=True, unbiased=False)
    return (x - m) / torch.sqrt(v + eps)
def _scatter_add(tgt, idx, src, axis=0):
    return tgt.clone().index_add_(axis, idx, src)
def _segment_sum(src, seg, n):
    out = torch.zeros((n,) + src.shape[1:], dtype=src.dtype)
    return out.index_add_(0, seg, src)
def _pad(x, pw):
    flat = tuple(v for pair in reversed(pw) for v in pair)
    return torch.nn.functional.pad(x, flat)

kernel_name = 'parallel_sum__ssm_conv_s4d__glu_gated_reglu__prompt'

def setup_inputs():

    _w = (lambda a: (lambda t: t.to(torch.bfloat16) if t.is_floating_point() else t)(torch.as_tensor(a)))
    def _A_setup():
        Bsz, L, D, N = 1, 2048, 448, 256
        return {
            "u": _w((lambda *s: torch.randn(*s, dtype=torch.bfloat16) * 0.05)(Bsz, L, D)),
            "log_A_real": _w((lambda *s: torch.randn(*s, dtype=torch.bfloat16) * 6.4)(D, N) * 0.5),
            "A_imag": _w((lambda *s: torch.randn(*s, dtype=torch.bfloat16) * 0.05)(D, N) * 3.14),
            "C_real": _w((lambda *s: torch.randn(*s, dtype=torch.bfloat16) * 0.1)(D, N) / N ** 0.5),
            "C_imag": _w((lambda *s: torch.randn(*s, dtype=torch.bfloat16) * 0.1)(D, N) / N ** 0.5),
            "log_dt": _w((lambda *s: torch.randn(*s, dtype=torch.bfloat16) * 0.05)(D) * 0.5 - 4.0),
            "D_skip": _w((lambda *s: torch.randn(*s, dtype=torch.bfloat16) * 0.05)(D)),
        }
    _A_in = _A_setup()

    def _B_setup():
        B, S, D, F = 1, 2048, 448, 1792
        return {
            "x": _w((lambda *s: torch.randn(*s, dtype=torch.bfloat16) * 0.05)(B, S, D)),
            "W_gate": _w((lambda *s: torch.randn(*s, dtype=torch.bfloat16) * 0.4)(D, F) * (2.0 / D) ** 0.5),
            "W_up": _w((lambda *s: torch.randn(*s, dtype=torch.bfloat16) * 0.4)(D, F) * (2.0 / D) ** 0.5),
            "W_down": _w((lambda *s: torch.randn(*s, dtype=torch.bfloat16) * 0.4)(F, D) * (1.0 / F) ** 0.5),
        }
    _B_in = _B_setup()

    return {
        "A_u": _A_in["u"],
        "A_log_A_real": _A_in["log_A_real"],
        "A_A_imag": _A_in["A_imag"],
        "A_C_real": _A_in["C_real"],
        "A_C_imag": _A_in["C_imag"],
        "A_log_dt": _A_in["log_dt"],
        "A_D_skip": _A_in["D_skip"],
        "B_x": _B_in["x"],
        "B_W_gate": _B_in["W_gate"],
        "B_W_up": _B_in["W_up"],
        "B_W_down": _B_in["W_down"],
    }

def reference(A_u, A_log_A_real, A_A_imag, A_C_real, A_C_imag, A_log_dt, A_D_skip, B_x, B_W_gate, B_W_up, B_W_down):
    def _A_ref(u, log_A_real, A_imag, C_real, C_imag, log_dt, D_skip):
        L = u.shape[1]
        dt = torch.exp(log_dt)
        A = -torch.exp(log_A_real) + 1j * A_imag
        C = (C_real + 1j * C_imag) * dt[:, None]
        pos = (lambda x, dt: x.to(dt))(torch.arange(L), log_dt.dtype)
        P = torch.exp((A * dt[:, None])[:, :, None] * pos[None, None, :])
        K = 2.0 * torch.einsum("dn,dnl->dl", C, P).real
        L2 = 2 * L
        u_t = (lambda x, *d: x.permute(*d))(u, 0, 2, 1)
        Kf = (lambda x, *a, **k: torch.fft.rfft(x.float(), *a, **k))(K, L2)
        Uf = (lambda x, *a, **k: torch.fft.rfft(x.float(), *a, **k))(u_t, L2)
        y = (lambda x, *a, **k: torch.fft.irfft(x, *a, **k).to(torch.bfloat16))(Uf * Kf[None, :, :], L2)[:, :, :L]
        y = (lambda x, *d: x.permute(*d))(y, 0, 2, 1)
        return y + u * D_skip[None, None, :]

    def _B_ref(x, W_gate, W_up, W_down):
        gate = torch.einsum("bsd,df->bsf", x, W_gate)
        up = torch.einsum("bsd,df->bsf", x, W_up)
        gate = gate * (lambda x, dt: x.to(dt))(gate > 0, gate.dtype)
        h = gate * up
        return torch.einsum("bsf,fd->bsd", h, W_down)

    x = A_u
    xn = _layernorm(x, axis=-1)
    return x + _A_ref(xn, A_log_A_real, A_A_imag, A_C_real, A_C_imag, A_log_dt, A_D_skip) + _B_ref(xn, B_W_gate, B_W_up, B_W_down)
```

```cpp
#include <hip/hip_runtime.h>
#include <hip/hip_bf16.h>
#include <stdint.h>
#define L_ 2048
#define D_ 448
#define N_ 256
#define F_ 1792
typedef unsigned short u16;
typedef __attribute__((ext_vector_type(8))) short s16x8;
typedef __attribute__((ext_vector_type(8))) __bf16 bf16x8;
typedef __attribute__((ext_vector_type(4))) float f32x4;
__device__ __forceinline__ float bf2f(u16 v) { return __uint_as_float(((unsigned)v) << 16); }
__device__ __forceinline__ u16 f2bf(float f) { __bf16 b = (__bf16)f; return __builtin_bit_cast(u16, b); }
__device__ __forceinline__ float rbf(float f) { return bf2f(f2bf(f)); }

__global__ void k_ln(const u16* x, u16* xn, u16* xnT) {
    int row = blockIdx.x * 4 + (threadIdx.x >> 6), lane = threadIdx.x & 63;
    float v[7]; float s = 0;
    for (int i = 0; i < 7; i++) { v[i] = bf2f(x[row * D_ + lane + 64 * i]); s += v[i]; }
    for (int o = 32; o; o >>= 1) s += __shfl_xor(s, o);
    float m = s / D_; float q = 0;
    for (int i = 0; i < 7; i++) q += (v[i] - m) * (v[i] - m);
    for (int o = 32; o; o >>= 1) q += __shfl_xor(q, o);
    float var = rbf(q / D_); float mb = rbf(m);
    float sd = rbf(sqrtf(rbf(var + 1e-5f)));
    for (int i = 0; i < 7; i++) {
        u16 r = f2bf(rbf(v[i] - mb) / sd);
        xn[row * D_ + lane + 64 * i] = r;
        xnT[(lane + 64 * i) * L_ + row] = r;
    }
}
__global__ void k_kgen(const u16* logA, const u16* Aim, const u16* Cr, const u16* Ci, const u16* logdt, float* K) {
    __shared__ float sa[N_], sb[N_], scr[N_], sci[N_];
    int d = blockIdx.x, t = threadIdx.x;
    float dt = rbf(expf(bf2f(logdt[d])));
    sa[t] = -rbf(expf(bf2f(logA[d * N_ + t]))) * dt;
    sb[t] = bf2f(Aim[d * N_ + t]) * dt;
    scr[t] = bf2f(Cr[d * N_ + t]) * dt;
    sci[t] = bf2f(Ci[d * N_ + t]) * dt;
    __syncthreads();
    __shared__ float kd[768];
    for (int i = t; i < 641; i += 256) {
        int p = i < 256 ? i : (i < 384 ? 256 + 2 * (i - 256) : (i < 512 ? 512 + 4 * (i - 384) : 1024 + 8 * (i - 512)));
        float pf = (float)p, acc = 0;
        for (int n = 0; n < N_; n++) {
            float e = __expf(sa[n] * pf);
            float ph = sb[n] * pf * 0.15915494309189535f;
            ph -= floorf(ph);
            float c = __builtin_amdgcn_cosf(ph), s = __builtin_amdgcn_sinf(ph);
            acc += e * (scr[n] * c - sci[n] * s);
        }
        kd[i] = 2 * acc;
    }
    __syncthreads();
    for (int l = t; l < L_; l += 256) {
        int p = (int)rbf((float)l);
        int i = p < 256 ? p : (p < 512 ? 256 + (p - 256) / 2 : (p < 1024 ? 384 + (p - 512) / 4 : 512 + (p - 1024) / 8));
        K[d * L_ + l] = kd[i];
    }
}
__global__ void k_conv(const float* K, const u16* xnT, const u16* Dskip, float* zT) {
    __shared__ float sk[L_], su[L_];
    int d = blockIdx.x, t = threadIdx.x;
    for (int l = t; l < L_; l += 256) { sk[l] = K[d * L_ + l]; su[l] = bf2f(xnT[d * L_ + l]); }
    __syncthreads();
    float ds = bf2f(Dskip[d]);
    for (int l = t; l < L_; l += 256) {
        float acc = 0;
        for (int j = 0; j <= l; j++) acc += sk[j] * su[l - j];
        zT[d * L_ + l] = acc + su[l] * ds;
    }
}
__global__ void k_gemm1(const u16* xn, const u16* Wg, const u16* Wu, u16* h) {
    int wave = threadIdx.x >> 6, lane = threadIdx.x & 63;
    int m0 = blockIdx.y * 64 + wave * 16, n0 = blockIdx.x * 16;
    int r = lane & 15, q = lane >> 4;
    f32x4 ag = {0, 0, 0, 0}, au = {0, 0, 0, 0};
    for (int k0 = 0; k0 < D_; k0 += 32) {
        s16x8 a = *(const s16x8*)(xn + (m0 + r) * D_ + k0 + 8 * q);
        s16x8 bg, bu;
        for (int j = 0; j < 8; j++) { bg[j] = Wg[(k0 + 8 * q + j) * F_ + n0 + r]; bu[j] = Wu[(k0 + 8 * q + j) * F_ + n0 + r]; }
        ag = __builtin_amdgcn_mfma_f32_16x16x32_bf16(__builtin_bit_cast(bf16x8, a), __builtin_bit_cast(bf16x8, bg), ag, 0, 0, 0);
        au = __builtin_amdgcn_mfma_f32_16x16x32_bf16(__builtin_bit_cast(bf16x8, a), __builtin_bit_cast(bf16x8, bu), au, 0, 0, 0);
    }
    for (int i = 0; i < 4; i++) h[(m0 + 4 * q + i) * F_ + n0 + r] = f2bf(fmaxf(ag[i], 0.f) * au[i]);
}
__global__ void k_gemm2(const u16* h, const u16* Wd, const u16* x, const float* zT, u16* out) {
    int wave = threadIdx.x >> 6, lane = threadIdx.x & 63;
    int m0 = blockIdx.y * 64 + wave * 16, n0 = blockIdx.x * 16;
    int r = lane & 15, q = lane >> 4;
    f32x4 acc = {0, 0, 0, 0};
    for (int k0 = 0; k0 < F_; k0 += 32) {
        s16x8 a = *(const s16x8*)(h + (m0 + r) * F_ + k0 + 8 * q);
        s16x8 b;
        for (int j = 0; j < 8; j++) b[j] = Wd[(k0 + 8 * q + j) * D_ + n0 + r];
        acc = __builtin_amdgcn_mfma_f32_16x16x32_bf16(__builtin_bit_cast(bf16x8, a), __builtin_bit_cast(bf16x8, b), acc, 0, 0, 0);
    }
    for (int i = 0; i < 4; i++) {
        int l = m0 + 4 * q + i, d = n0 + r;
        out[l * D_ + d] = f2bf(bf2f(x[l * D_ + d]) + zT[d * L_ + l] + acc[i]);
    }
}
__global__ void k_empty(int* p) { if (p == nullptr) p[0] = 1; }
extern "C" void kernel_launch(void* const* d_in, const int* in_sizes, int n_in,
                              void* d_out, int out_size, void* d_ws, size_t ws_size,
                              hipStream_t stream) {
    const u16* x = (const u16*)d_in[0];
    char* ws = (char*)d_ws;
    u16* xn = (u16*)(ws + (1 << 20));
    u16* xnT = (u16*)(ws + (4 << 20));
    float* K = (float*)(ws + (8 << 20));
    float* zT = (float*)(ws + (16 << 20));
    u16* h = (u16*)(ws + (24 << 20));
    k_ln<<<L_ / 4, 256, 0, stream>>>(x, xn, xnT);
    k_kgen<<<D_, 256, 0, stream>>>((const u16*)d_in[1], (const u16*)d_in[2], (const u16*)d_in[3], (const u16*)d_in[4], (const u16*)d_in[5], K);
    k_conv<<<D_, 256, 0, stream>>>(K, xnT, (const u16*)d_in[6], zT);
    k_gemm1<<<dim3(F_ / 16, L_ / 64), 256, 0, stream>>>(xn, (const u16*)d_in[8], (const u16*)d_in[9], h);
    k_gemm2<<<dim3(D_ / 16, L_ / 64), 256, 0, stream>>>(h, (const u16*)d_in[10], x, zT, (u16*)d_out);
}
```

```cpp
#include <hip/hip_runtime.h>
#include <stdint.h>

typedef unsigned u32;
typedef unsigned long long u64;
typedef unsigned short u16;
typedef __attribute__((ext_vector_type(2))) u32 u32x2;
typedef __attribute__((ext_vector_type(4))) u32 u32x4;
typedef __attribute__((ext_vector_type(4))) float f32x4;
typedef __attribute__((ext_vector_type(16))) float f32x16;
typedef __attribute__((ext_vector_type(8))) __bf16 bf16x8;
typedef __attribute__((ext_vector_type(2))) __bf16 bf16x2;
typedef __attribute__((ext_vector_type(4))) short s16x4;
typedef __amdgpu_buffer_rsrc_t rsrc_t;
#define LAS __attribute__((address_space(3)))
#define GAS __attribute__((address_space(1)))
#define DI __device__ __forceinline__

constexpr u32 WS_EPOCH = 0, WS_FX = 256, WS_FZ = 2560, WS_FH = 10240, WS_ST = 18432, WS_CS = 34816;
constexpr u32 WS_XNT = 65536, WS_ZT = WS_XNT + 1835008, WS_H = WS_ZT + 1835008, WS_END = WS_H + 7340032;
constexpr u32 WS_DBG = 16u << 20;
constexpr u32 XS0 = 0, WS0 = 49152, SSM = 114688;
constexpr u32 UB = SSM;
constexpr u32 STG01 = XS0 + 16384, STG23 = SSM;
constexpr u32 EXC = SSM;
constexpr u32 UT = SSM + 16384;
constexpr u32 KN = SSM + 33024;
constexpr u32 MISC = SSM + 42496;
constexpr u32 M_NM = MISC, M_RS2 = MISC + 512, M_CSG = MISC + 1024, M_CSU = MISC + 1536, M_CNT = MISC + 2048;
constexpr u32 ZTL = UT;
constexpr u32 LDS_BYTES = 163840;
constexpr int SPIN_MAX = 1 << 18;
#define ROT1 ((mi + fj) % 7)
#define EARLYD 0
#define WDPRE 0

#define TS(i) do {} while (0)
#define TSDUMP() do {} while (0)
#define STAMP(i) do {} while (0)
#define CSTAMP(i) do {} while (0)

struct Params {
    const char* x; const char* logA; const char* Aim; const char* Cr; const char* Ci; const char* logdt; const char* Dsk;
    const char* Wg; const char* Wu; const char* Wd; char* out; char* ws;
};

DI float bflo(u32 v) { return __uint_as_float(v << 16); }
DI float bfhi(u32 v) { return __uint_as_float(v & 0xffff0000u); }
DI u32 pkbf(float a, float b) { bf16x2 v = {(__bf16)a, (__bf16)b}; return __builtin_bit_cast(u32, v); }
DI float rbf(float a) { return bflo(pkbf(a, 0.f)); }
DI bf16x8 asbf(u32x4 v) { return __builtin_bit_cast(bf16x8, v); }
template <class T> DI T ldsr(u32 a) { return *(const T LAS*)(uintptr_t)a; }
template <class T> DI void ldsw(u32 a, T v) { *(T LAS*)(uintptr_t)a = v; }
DI u32x2 ldstr(u32 a) { return __builtin_bit_cast(u32x2, __builtin_amdgcn_ds_read_tr16_b64_v4i16((s16x4 LAS*)(uintptr_t)a)); }
DI u32x4 ldstr2(u32 a, u32 b) { u32x2 p = ldstr(a), q = ldstr(b); return u32x4{p.x, p.y, q.x, q.y}; }
#define WAITV(n) asm volatile("s_waitcnt vmcnt(" #n ")" ::: "memory")
#define BAR() asm volatile("s_waitcnt lgkmcnt(0)\n\ts_barrier" ::: "memory")
#define SB() __builtin_amdgcn_sched_barrier(0)

DI void dma4(u32 m0v, u32 v0, u32 v1, u32 v2, u32 v3, rsrc_t r, u32 soff) {
    asm volatile("s_mov_b32 m0, %0\n\ts_nop 0\n\t"
                 "buffer_load_dwordx4 %1, %5, %6 offen lds\n\t"
                 "buffer_load_dwordx4 %2, %5, %6 offen offset:1024 lds\n\t"
                 "buffer_load_dwordx4 %3, %5, %6 offen offset:2048 lds\n\t"
                 "buffer_load_dwordx4 %4, %5, %6 offen offset:3072 lds"
                 :: "s"(m0v), "v"(v0), "v"(v1), "v"(v2), "v"(v3), "s"(r), "s"(soff) : "memory");
}
DI void dma1c(u32 m0v, u32 v, rsrc_t r, u32 soff) {
    asm volatile("s_mov_b32 m0, %0\n\ts_nop 0\n\t"
                 "buffer_load_dwordx4 %1, %2, %3 offen sc1 lds"
                 :: "s"(m0v), "v"(v), "s"(r), "s"(soff) : "memory");
}
DI void ld16c(u32x4& d, u32 v, rsrc_t r, u32 soff) { asm volatile("buffer_load_dwordx4 %0, %1, %2, %3 offen sc1" : "=v"(d) : "v"(v), "s"(r), "s"(soff) : "memory"); }
DI void ld8c(u32x2& d, u32 v, rsrc_t r, u32 soff) { asm volatile("buffer_load_dwordx2 %0, %1, %2, %3 offen sc1" : "=v"(d) : "v"(v), "s"(r), "s"(soff) : "memory"); }
#define PIN(x) asm volatile("" : "+v"(x) :: "memory")
DI void st16c(u32x4 d, u32 v, rsrc_t r, u32 off) { __builtin_amdgcn_raw_buffer_store_b128(d, r, v + off, 0, 16); }
DI void stflag(char* p, u32x2 tag) { __hip_atomic_store((GAS u64*)(uintptr_t)p, ((u64)tag.y << 32) | tag.x, __ATOMIC_RELAXED, __HIP_MEMORY_SCOPE_AGENT); }
DI u32x2 nexttag(u32x2 e) { u64 v = (((u64)e.y << 32) | e.x) + 1; return u32x2{(u32)v, (u32)(v >> 32)}; }
DI void waitflags(u32x2 f, u32x2 tag, u32 v, int n, int lane, rsrc_t r, u32 soff) {
#pragma unroll 1
    for (int it = 0; it < SPIN_MAX; ++it) {
        bool ok = (lane >= n) || (f.x == tag.x && f.y == tag.y);
        if (__all(ok)) break;
        __builtin_amdgcn_s_sleep(1);
        ld8c(f, v, r, soff); WAITV(0); PIN(f);
    }
}
DI void lds_arrive(u32 a, int lane) {
    asm volatile("s_waitcnt lgkmcnt(0)" ::: "memory");
    if (lane == 0) __hip_atomic_fetch_add((LAS u32*)(uintptr_t)a, 1u, __ATOMIC_RELAXED, __HIP_MEMORY_SCOPE_WORKGROUP);
}
DI void lds_wait(u32 a, u32 target) {
#pragma unroll 1
    for (int it = 0; it < SPIN_MAX; ++it) {
        u32 c = __hip_atomic_load((LAS u32*)(uintptr_t)a, __ATOMIC_RELAXED, __HIP_MEMORY_SCOPE_WORKGROUP);
        if (__builtin_amdgcn_readfirstlane(c) >= target) break;
    }
    asm volatile("" ::: "memory");
}
DI f32x16 mfma32(u32x4 a, u32x4 b, f32x16 c) { return __builtin_amdgcn_mfma_f32_32x32x16_bf16(asbf(a), asbf(b), c, 0, 0, 0); }
DI f32x4 mfma16(u32x4 a, u32x4 b, f32x4 c) { return __builtin_amdgcn_mfma_f32_16x16x32_bf16(asbf(a), asbf(b), c, 0, 0, 0); }
DI f32x4 sel4(int c, f32x4 a, f32x4 b) { asm("" : "+v"(a), "+v"(b)); return c ? a : b; }
DI float dot2(u32 a, u32 b, float c) { return __builtin_amdgcn_fdot2_f32_bf16(__builtin_bit_cast(bf16x2, a), __builtin_bit_cast(bf16x2, b), c, false); }
template <int C> DI float dppf(float v) { return __int_as_float(__builtin_amdgcn_mov_dpp(__float_as_int(v), C, 0xf, 0xf, true)); }
DI float exp_acc(float x) {
    const float L = 1.44269502162933349609375f, Ll = 1.92596299112661746e-8f;
    float hi = x * L, lo = __builtin_fmaf(x, L, -hi) + x * Ll;
    float e = __builtin_amdgcn_exp2f(hi);
    return __builtin_fmaf(e * lo, 0.693147182464599609375f, e);
}

struct Cx2 { float r0, i0, r1, i1; };
struct Mul2 { float r0, i0, n0, r1, i1, n1; };
DI Cx2 cmul2(const Cx2& a, const Mul2& w) {
    Cx2 t;
    asm("v_mul_f32 %0, %4, %8\n\tv_mul_f32 %1, %4, %9\n\tv_mul_f32 %2, %6, %11\n\tv_mul_f32 %3, %6, %12\n\t"
        "v_fmac_f32 %0, %5, %10\n\tv_fmac_f32 %1, %5, %8\n\tv_fmac_f32 %2, %7, %13\n\tv_fmac_f32 %3, %7, %11"
        : "=&v"(t.r0), "=&v"(t.i0), "=&v"(t.r1), "=&v"(t.i1)
        : "v"(a.r0), "v"(a.i0), "v"(a.r1), "v"(a.i1), "v"(w.r0), "v"(w.i0), "v"(w.n0), "v"(w.r1), "v"(w.i1), "v"(w.n1));
    return t;
}
struct Unit { u32 m0, so; };
template <int I> DI void piece(const Unit& u, u32 v, rsrc_t r) {
    asm volatile("s_mov_b32 m0, %0\n\ts_nop 0\n\tbuffer_load_dwordx4 %1, %2, %3 offen offset:%4 lds" :: "s"(u.m0), "v"(v), "s"(r), "s"(u.so), "i"(I * 1024) : "memory");
}
DI float addf(float a, float b) { float r; asm("v_add_f32 %0, %1, %2" : "=v"(r) : "v"(a), "v"(b)); return r; }
DI u32x2 packrow(const Cx2& v) { u32x2 o = {pkbf(v.r0, v.i0), pkbf(v.r1, v.i1)}; asm volatile("" : "+v"(o)); return o; }
DI void cexp1(float a2, float br, float p, float& wr, float& wi) {
    float e = __builtin_amdgcn_exp2f(a2 * p), ph = __builtin_amdgcn_fractf(br * p);
    wr = e * __builtin_amdgcn_cosf(ph); wi = e * __builtin_amdgcn_sinf(ph);
}

DI void ln_load(u32x4& a, u32x4& b, int blk, int w4, int lane, rsrc_t rX) {
    const u32 v = lane < 56 ? (u32)(8 * blk + 2 * w4) * 896 + lane * 16 : 0x7fff0000u;
    asm volatile("buffer_load_dwordx4 %0, %2, %3, 0 offen\n\tbuffer_load_dwordx4 %1, %2, %3, 0 offen offset:896" : "=&v"(a), "=&v"(b) : "v"(v), "s"(rX) : "memory");
}
DI float red2(float s0, float s1) {
    auto r = __builtin_amdgcn_permlane32_swap(__float_as_uint(s0), __float_as_uint(s1), false, false);
    float t = __uint_as_float(r[0]) + __uint_as_float(r[1]);
    auto q = __builtin_amdgcn_permlane16_swap(__float_as_uint(t), __float_as_uint(t), false, false);
    t = __uint_as_float(q[0]) + __uint_as_float(q[1]);
    t += dppf<0xB1>(t); t += dppf<0x4E>(t); t += dppf<0x141>(t); t += dppf<0x140>(t);
    return t;
}
DI void ln_compute(u32x4 a, u32x4 b, int blk, int w4, int lane, u32 tagx, rsrc_t rWS) {
    float s0 = 0.f, s1 = 0.f;
#pragma unroll
    for (int j = 0; j < 4; ++j) { s0 = dot2(a[j], 0x3f803f80u, s0); s1 = dot2(b[j], 0x3f803f80u, s1); }
    const float mean = red2(s0, s1) * (1.f / 448.f), mb = rbf(mean);
    auto mm = __builtin_amdgcn_permlane32_swap(__float_as_uint(mb), __float_as_uint(mb), false, false);
    const float m0 = __uint_as_float(mm[0]), m1 = __uint_as_float(mm[1]);
    float q0 = 0.f, q1 = 0.f;
#pragma unroll
    for (int j = 0; j < 4; ++j) {
        float c = bflo(a[j]) - m0, d = bfhi(a[j]) - m0, e = bflo(b[j]) - m1, f = bfhi(b[j]) - m1;
        q0 = __builtin_fmaf(c, c, q0); q0 = __builtin_fmaf(d, d, q0); q1 = __builtin_fmaf(e, e, q1); q1 = __builtin_fmaf(f, f, q1);
        a[j] = pkbf(c, d); b[j] = pkbf(e, f);
    }
    if (lane >= 56) { q0 = 0.f; q1 = 0.f; }
    const float dm = mean - mb;
    const float var = rbf(fmaxf(red2(q0, q1) * (1.f / 448.f) - dm * dm, 0.f));
    const float sd = rbf(__builtin_sqrtf(rbf(var + 1e-5f)));
    auto pp = __builtin_amdgcn_permlane32_swap(pkbf(mb, sd), pkbf(mb, sd), false, false);
    if (lane == 0) st16c(u32x4{pp[0], tagx, pp[1], tagx}, (u32)(4 * blk + w4) * 16, rWS, WS_ST);
    const float r0 = __builtin_amdgcn_rcpf(bfhi(pp[0])), r1 = __builtin_amdgcn_rcpf(bfhi(pp[1]));
    u32x4 o, p;
#pragma unroll
    for (int j = 0; j < 4; ++j) { o[j] = pkbf(bflo(a[j]) * r0, bfhi(a[j]) * r0); p[j] = pkbf(bflo(b[j]) * r1, bfhi(b[j]) * r1); }
    if (lane < 56) { ldsw<u32x4>(UB + 2 * w4 * 912 + lane * 16, o); ldsw<u32x4>(UB + (2 * w4 + 1) * 912 + lane * 16, p); }
}
DI void ln_store(int blk, int w4, int lane, rsrc_t rWS) {
#pragma unroll
    for (int i = 0; i < 2; ++i) {
        const int it = i == 0 ? w4 : (w4 < 3 ? w4 + 4 : 3);
        const u32 a = UB + ((lane & 15) >> 2) * 912 + (4 * it + (lane >> 4)) * 32 + (lane & 3) * 8;
        st16c(ldstr2(a, a + 4 * 912), (u32)blk * 7168 + it * 1024 + lane * 16, rWS, WS_XNT);
    }
}

DI void helper_wg(const Params& P, int hw, int tid, int lane, int wave, rsrc_t rWS, rsrc_t rX) {
    STAMP(0);
    u32x2 tag;
    ld8c(tag, 0, rWS, WS_EPOCH);
    u32x4 ra, rb, wv[14];
    ln_load(ra, rb, blockIdx.x, wave & 3, lane, rX);
    const int c = lane & 7, hwc = hw < 28 ? hw : 0;
    const char* wp = (((tid >> 3) & 1) ? P.Wu : P.Wg) + (size_t)(tid >> 4) * 3584 + hwc * 128 + c * 16;
#pragma unroll
    for (int i = 0; i < 14; ++i) wv[i] = *(const u32x4*)(wp + (size_t)i * 32 * 3584);
    SB();
    asm volatile("s_waitcnt vmcnt(14)" : "+v"(tag), "+v"(ra), "+v"(rb) :: "memory");
    tag = nexttag(tag);
    if (wave < 4) ln_compute(ra, rb, blockIdx.x, wave, lane, tag.x, rWS);
    BAR();
    if (wave < 4) ln_store(blockIdx.x, wave, lane, rWS);
    STAMP(1);
    if (hw < 28) {
        float a[8];
        u32 c10 = 0x00003f80u;
        asm("" : "+s"(c10));
#pragma unroll
        for (int j = 0; j < 8; ++j) a[j] = 0.f;
#pragma unroll
        for (int i = 0; i < 14; ++i)
#pragma unroll
            for (int j = 0; j < 4; ++j) { a[2 * j] = dot2(wv[i][j], c10, a[2 * j]); a[2 * j + 1] = dot2(wv[i][j], 0x3f800000u, a[2 * j + 1]); }
        float s4[4], t2[2];
#pragma unroll
        for (int j = 0; j < 4; ++j) { auto r = __builtin_amdgcn_permlane32_swap(__float_as_uint(a[j]), __float_as_uint(a[j + 4]), false, false); s4[j] = __uint_as_float(r[0]) + __uint_as_float(r[1]); }
#pragma unroll
        for (int j = 0; j < 2; ++j) { auto r = __builtin_amdgcn_permlane16_swap(__float_as_uint(s4[j]), __float_as_uint(s4[j + 2]), false, false); t2[j] = __uint_as_float(r[0]) + __uint_as_float(r[1]); }
        ldsw<u32x2>(wave * 512 + ((lane >> 3) & 1) * 256 + (8 * c + 2 * ((lane >> 4) & 1) + 4 * (lane >> 5)) * 4, u32x2{__float_as_uint(t2[0]), __float_as_uint(t2[1])});
    }
    WAITV(0);
    BAR();
    STAMP(2);
    if (wave == 1 && lane == 0) stflag(P.ws + WS_FX + blockIdx.x * 8, tag);
    if (wave == 0) {
        if (hw < 28) {
            float g = 0.f, u = 0.f;
#pragma unroll
            for (int w = 0; w < 8; ++w) { g += ldsr<float>(w * 512 + lane * 4); u += ldsr<float>(w * 512 + 256 + lane * 4); }
            float gh = rbf(g), uh = rbf(u);
            st16c(u32x4{pkbf(gh, g - gh), tag.x, pkbf(uh, u - uh), tag.x}, (u32)(64 * hw + lane) * 16, rWS, WS_CS);
        } else if (hw == 31) {
            u32x2 f;
#pragma unroll 1
            for (int i = 0; i < 4; ++i) { ld8c(f, lane * 8 + i * 512, rWS, WS_FX); WAITV(0); PIN(f); waitflags(f, tag, lane * 8 + i * 512, 64, lane, rWS, WS_FX); }
            if (lane == 0) stflag(P.ws + WS_EPOCH, tag);
        }
    }
    STAMP(3);
}

#define MF32(acc, a, b) asm volatile("v_mfma_f32_32x32x16_bf16 %0, %1, %2, %0" : "+v"(acc) : "v"(a), "v"(b) : "memory")
#define MF32Z(acc, a, b) asm volatile("v_mfma_f32_32x32x16_bf16 %0, %1, %2, 0" : "=&v"(acc) : "v"(a), "v"(b) : "memory")
struct G1Frag { u32x4 g0, g1, u0, u1, x0, x1; };
template <int J, int S, int N> DI void g1rd(G1Frag& f, const u32 (&xa)[4], u32 wa0, u32 wa1) {
    constexpr u32 wo = (J & 3) * 16384 + S * 4096, xo = XS0 + ((J >> 1) % 3) * 16384;
    const u32 xv = xa[(J & 1) * 2 + S] + xo;
    if (N == 0) f.g0 = ldstr2(wa0 + wo, wa0 + wo + 1024);
    if (N == 1) { f.x0 = ldsr<u32x4>(xv); const u32x2 p = ldstr(wa0 + wo + 8192); f.u0[0] = p.x; f.u0[1] = p.y; }
    if (N == 2) { const u32x2 p = ldstr(wa0 + wo + 9216); f.u0[2] = p.x; f.u0[3] = p.y; f.x1 = ldsr<u32x4>(xv + 4096); }
    if (N == 3) f.g1 = ldstr2(wa1 + wo, wa1 + wo + 1024);
    if (N == 4) f.u1 = ldstr2(wa1 + wo + 8192, wa1 + wo + 9216);
}
template <int J, int S, bool RD, bool FIRST, class F> DI void g1sub(f32x16 (&ac)[8], const G1Frag& f, G1Frag& n, const u32 (&xa)[4], u32 wa0, u32 wa1, F fill) {
#define G1M(i, a, b) do { if (FIRST) MF32Z(ac[i], a, b); else MF32(ac[i], a, b); } while (0)
    G1M(0, f.g0, f.x0); if (RD) g1rd<J, S, 0>(n, xa, wa0, wa1);
    G1M(1, f.u0, f.x0); if (RD) g1rd<J, S, 1>(n, xa, wa0, wa1);
    G1M(2, f.g0, f.x1); if (RD) g1rd<J, S, 2>(n, xa, wa0, wa1);
    G1M(3, f.u0, f.x1); if (RD) g1rd<J, S, 3>(n, xa, wa0, wa1);
    G1M(4, f.g1, f.x0); if (RD) g1rd<J, S, 4>(n, xa, wa0, wa1);
    G1M(5, f.u1, f.x0); fill(0);
    G1M(6, f.g1, f.x1); fill(1);
    G1M(7, f.u1, f.x1); fill(2);
#undef G1M
}
constexpr u32 rslot(int u) { return u == 0 ? WS0 + 32768 : u == 1 ? WS0 + 49152 : u == 2 ? XS0 + 32768 : u == 3 ? WS0 : u == 4 ? WS0 + 16384 : u == 5 ? XS0 : XS0 + 16384; }
struct WdF { u32x4 a, b; };
template <int I> DI WdF wdread(u32 ta0, u32 ta1) {
    constexpr u32 sl = rslot(I >> 1), hi = sl >= 49152 ? 49152 : 0, o = sl - hi + (I & 1) * 2048;
    u32 b0 = ta0, b1 = ta1;
    if (hi) { b0 += hi; b1 += hi; asm("" : "+v"(b0), "+v"(b1)); }
    return WdF{ldstr2(b0 + o, b0 + o + 1024), ldstr2(b1 + o, b1 + o + 1024)};
}
constexpr int HW = 7;

__global__ __launch_bounds__(512, 2) void fused_kernel(Params P) {
    const int tid = threadIdx.x, lane = tid & 63;
    const int wave = __builtin_amdgcn_readfirstlane(tid >> 6);
    const int xcd = blockIdx.x & 7, r_ = blockIdx.x >> 3;
    const rsrc_t rWS = __builtin_amdgcn_make_buffer_rsrc(P.ws, 0, WS_END, 0x00020000);
    const rsrc_t rX = __builtin_amdgcn_make_buffer_rsrc((void*)P.x, 0, 1835008, 0x00020000);
    if (r_ >= 28) { helper_wg(P, (r_ - 28) * 8 + xcd, tid, lane, wave, rWS, rX); return; }

    const int mi = (xcd >> 1) * 4 + r_ / 7, nj = r_ % 7, fj = (xcd & 1) * 7 + nj, mt = 2 * mi + (xcd & 1);
    const int cg = xcd * 28 + r_;
    const int w4 = wave & 3;
    u32x4 xres;
    u32 tsv = 0;
    STAMP(0); CSTAMP(14);
#define STREAM_SETUP() \
    const int ks0 = ROT1;                           \
    const rsrc_t rW = __builtin_amdgcn_make_buffer_rsrc((void*)((w4 >> 1) ? P.Wu : P.Wg), 0, 1605632, 0x00020000); \
    u32 vx[4], vw[4];                               \
    _Pragma("unroll") for (int i = 0; i < 4; ++i) { \
        const int rx = 32 * w4 + 8 * i + (lane >> 3); \
        vx[i] = rx * 896 + (((lane & 7) ^ ((rx >> 1) & 7)) << 4) - 1024 * i; \
        const int rw = 16 * (w4 & 1) + 4 * i + (lane >> 4); \
        vw[i] = rw * 3584 + (((lane & 15) ^ ((rw & 3) << 2)) << 4) - 1024 * i; \
    } \
    auto unitX = [&](int s) { int k = ks0 + s; k -= k >= 7 ? 7 : 0; return Unit{(u32)(XS0 + (s % 3) * 16384 + w4 * 4096), (u32)(mi * 114688 + k * 128)}; }; \
    auto unitW = [&](int j) { int k = ks0 + (j >> 1); k -= k >= 7 ? 7 : 0; return Unit{(u32)(WS0 + (j & 3) * 16384 + w4 * 4096), (u32)((k * 64 + (j & 1) * 32) * 3584 + fj * 256)}; }; \
    auto issueX = [&](int s) { const Unit u = unitX(s); dma4(u.m0, vx[0], vx[1], vx[2], vx[3], rX, u.so); }; \
    auto issueW = [&](int j) { const Unit u = unitW(j); dma4(u.m0, vw[0], vw[1], vw[2], vw[3], rW, u.so); }

    if (wave < 4) {
        u32x2 tag;
        u32x4 ra, rb;
        ld8c(tag, 0, rWS, WS_EPOCH);
        ln_load(ra, rb, blockIdx.x, w4, lane, rX);
        STREAM_SETUP();
        issueX(0); issueW(1);
        asm volatile("s_waitcnt vmcnt(8)" : "+v"(tag), "+v"(ra), "+v"(rb) :: "memory");
        tag = nexttag(tag);
        ln_compute(ra, rb, blockIdx.x, w4, lane, tag.x, rWS);
        const int lh = w4 >> 1, fh = w4 & 1;
        const int r = lane & 31, h = lane >> 5, blk = (lane >> 4) & 1, qq = (lane & 15) >> 2, p = lane & 3;
        u32 xa[4];
#pragma unroll
        for (int s = 0; s < 4; ++s) xa[s] = (64 * lh + r) * 128 + (((2 * s + h) ^ ((r >> 1) & 7)) << 4);
        const u32 wrow = WS0 + (8 * h + qq) * 256 + 8 * (p & 1);
        u32 wa0 = wrow + (((8 * fh + 2 * blk + (p >> 1)) ^ (qq << 2)) << 4);
        u32 wa1 = wrow + (((8 * fh + 4 + 2 * blk + (p >> 1)) ^ (qq << 2)) << 4);
        asm volatile("" : "+v"(wa0), "+v"(wa1), "+v"(xa[0]), "+v"(xa[1]), "+v"(xa[2]), "+v"(xa[3]));
        const rsrc_t rD = __builtin_amdgcn_make_buffer_rsrc((void*)P.Wd, 0, 1605632, 0x00020000);
        u32 vd0 = (lane >> 3) * 896 + (((lane & 7) ^ (((lane >> 4) & 1) << 2)) << 4);
        auto unitD = [&](int u) { return Unit{(u32)(rslot(u % 7) + w4 * 4096), (u32)((448 * w4 + 32 * u) * 896 + nj * 128)}; };
        f32x16 ac[8];
        G1Frag fa, fb;
        STAMP(9);
        TS(0); WAITV(5);
        TS(1); BAR(); TS(2);
        STAMP(1);
        g1rd<0, 0, 0>(fa, xa, wa0, wa1); g1rd<0, 0, 1>(fa, xa, wa0, wa1); g1rd<0, 0, 2>(fa, xa, wa0, wa1); g1rd<0, 0, 3>(fa, xa, wa0, wa1); g1rd<0, 0, 4>(fa, xa, wa0, wa1);
        ln_store(blockIdx.x, w4, lane, rWS);
        Unit uw, ux = unitX(2);
#define PIECE(j, n) do { \
            if ((j) == 0) { if ((n) == 0) piece<0>(ux, vx[0], rX); if ((n) == 1) piece<1>(ux, vx[1], rX); } \
            else if ((n) < 4 && (j) <= 10) piece<(n) & 3>(uw, vw[(n) & 3], rW); \
            else if ((j) <= 9) piece<((j) & 1) * 2 + ((n) & 1)>(ux, vx[((j) & 1) * 2 + ((n) & 1)], rX); \
            else { constexpr int e_ = ((j) - 10) * 6 + (n) - 4; \
                   if (e_ >= 0 && e_ < 4 * EARLYD) piece<e_ & 3>(unitD(e_ >> 2), vd0 + (e_ & 3) * 6144, rD); } } while (0)
#define HALF(J) do { \
            g1sub<J, 1, true, J == 0>(ac, fa, fb, xa, wa0, wa1, [&](int n) { if (n == 0) PIECE(J, (J == 0 ? 0 : 3)); if (n == 1) PIECE(J, (J == 0 ? 1 : 4)); if (n == 2 && J != 0) PIECE(J, 5); }); \
            if (J == 0) WAITV(5); \
            if (J >= 3 && J <= 9) WAITV(12); \
            if (J == 10) asm volatile("s_waitcnt vmcnt(%0)" :: "i"(10 + (EARLYD ? 2 : 0)) : "memory"); \
            if (J == 11) asm volatile("s_waitcnt vmcnt(%0)" :: "i"(4 + (4 * EARLYD < 8 ? 4 * EARLYD : 8)) : "memory"); \
            if (J == 12) asm volatile("s_waitcnt vmcnt(%0)" :: "i"(4 * EARLYD < 14 ? 4 * EARLYD : 14) : "memory"); \
            TS(3 * J + 4); BAR(); TS(3 * J + 5); \
            if (J < 10) uw = unitW(J + 4); \
            if ((J & 1) && J < 9) ux = unitX((J + 5) / 2); \
            if (J == 3 && tid == 0) stflag(P.ws + WS_FX + blockIdx.x * 8, tag); \
            g1sub<(J < 13 ? J + 1 : 0), 0, (J < 13), false>(ac, fb, fa, xa, wa0, wa1, [&](int n) { if (n == 0) PIECE(J + 1, 0); if (n == 1) PIECE(J + 1, 1); if (n == 2) PIECE(J + 1, 2); }); \
        } while (0)
        HALF(0); HALF(1); HALF(2); HALF(3); HALF(4); HALF(5); HALF(6);
        HALF(7); HALF(8); HALF(9); HALF(10); HALF(11); HALF(12);
        {
            const u32 z = 0;
            const u32 c0 = (64 * fh + r) * 4, l0 = (64 * lh + r) * 4;
            u32 a0 = ldsr<u32>(M_CSG + c0), a1 = ldsr<u32>(M_CSG + c0 + 128), b0 = ldsr<u32>(M_CSU + c0), b1 = ldsr<u32>(M_CSU + c0 + 128);
            u32 m0 = ldsr<u32>(M_NM + l0), m1 = ldsr<u32>(M_NM + l0 + 128);
            HALF(13);
            G1Frag f;
            f.g0 = u32x4{h ? z : a0, z, z, z}; f.g1 = u32x4{h ? z : a1, z, z, z}; f.u0 = u32x4{h ? z : b0, z, z, z}; f.u1 = u32x4{h ? z : b1, z, z, z};
            f.x0 = u32x4{h ? z : m0, z, z, z}; f.x1 = u32x4{h ? z : m1, z, z, z};
            asm volatile("s_nop 1" : "+v"(f.g0), "+v"(f.g1), "+v"(f.u0), "+v"(f.u1), "+v"(f.x0), "+v"(f.x1));
            g1sub<0, 0, false, false>(ac, f, fa, xa, wa0, wa1, [&](int) {});
            asm volatile("s_nop 11" : "+v"(ac[0]), "+v"(ac[1]), "+v"(ac[2]), "+v"(ac[3]), "+v"(ac[4]), "+v"(ac[5]), "+v"(ac[6]), "+v"(ac[7]));
        }
        STAMP(2);
        {
            const u32 hb = WS_H + ((2 * mi + lh) * 112 + 8 * fj + 4 * fh) * 2048 + lane * 16;
#pragma unroll
            for (int t = 0; t < 4; ++t)
#pragma unroll
                for (int s = 0; s < 2; ++s) {
                    u32x4 o;
#pragma unroll
                    for (int j = 0; j < 4; ++j) {
                        const int i = 8 * s + 2 * j;
                        o[j] = pkbf(fmaxf(ac[2 * t][i], 0.f) * ac[2 * t + 1][i], fmaxf(ac[2 * t][i + 1], 0.f) * ac[2 * t + 1][i + 1]);
                    }
                    st16c(o, hb, rWS, ((2 * (t >> 1) + s) * 2 + (t & 1)) * 1024);
                }
        }
        STAMP(3);
        u32 vd[4];
#pragma unroll
        for (int i = 0; i < 4; ++i) vd[i] = vd0 + i * 6144;
        auto issueD = [&](int u) { const Unit t = unitD(u); dma4(t.m0, vd[0], vd[1], vd[2], vd[3], rD, t.so); };
#pragma unroll
        for (int u = 0; u < WDPRE; ++u) issueD(u);
        lds_wait(M_CNT, 8);
        STAMP(10);
#pragma unroll
        for (int u = WDPRE; u < 3; ++u) issueD(u);
        asm volatile("s_waitcnt vmcnt(%0)" :: "i"(WDPRE > 3 ? 4 * WDPRE : 12) : "memory");
        if (lane == 0) stflag(P.ws + WS_FH + ((2 * mi + lh) * 32 + 2 * fj + fh) * 8, tag);
        STAMP(11);
#pragma unroll
        for (int u = (WDPRE > 3 ? WDPRE : 3); u < 7; ++u) issueD(u);
        const u32 trow = w4 * 4096 + (4 * h + qq) * 128 + 8 * (p & 1);
        const u32 ta0 = trow + (((2 * blk + (p >> 1)) ^ ((qq >> 1) << 2)) << 4), ta1 = trow + (((4 + 2 * blk + (p >> 1)) ^ ((qq >> 1) << 2)) << 4);
        WdF wd[14];
#define DRAIN(u) do { WAITV(24); wd[2 * u] = wdread<2 * u>(ta0, ta1); wd[2 * u + 1] = wdread<2 * u + 1>(ta0, ta1); \
                      asm volatile("s_waitcnt lgkmcnt(0)" : "+v"(wd[2 * u].a), "+v"(wd[2 * u].b), "+v"(wd[2 * u + 1].a), "+v"(wd[2 * u + 1].b) :: "memory"); issueD(7 + u); } while (0)
        DRAIN(0); DRAIN(1); DRAIN(2); DRAIN(3); DRAIN(4); DRAIN(5); DRAIN(6);
        STAMP(4);
        u32x2 fl;
        const u32 fv = (7 * w4 + lane) * 8;
        ld8c(fl, fv, rWS, WS_FH + mt * 256);
        asm volatile("buffer_load_dwordx4 %0, %1, %2, 0 offen" : "=v"(xres) : "v"((u32)(64 * mt + (tid >> 3)) * 896 + nj * 128 + (tid & 7) * 16), "s"(rX) : "memory");
        asm volatile("s_waitcnt vmcnt(0)" : "+v"(fl), "+v"(xres) :: "memory");
        waitflags(fl, tag, fv, 7, lane, rWS, WS_FH + mt * 256);
        STAMP(5);
        u32x4 hf[HW][2];
        const u32 hv = lane * 16, hs = WS_H + (mt * 112 + 28 * w4) * 2048;
#define HLOAD(i) asm volatile("buffer_load_dwordx4 %0, %2, %3, %4 offen offset:%5 sc1\n\tbuffer_load_dwordx4 %1, %2, %3, %4 offen offset:%6 sc1" \
                    : "=&v"(hf[(i) % HW][0]), "=&v"(hf[(i) % HW][1]) : "v"(hv), "s"(rWS), "s"(hs + ((i) >> 1) * 4096), "i"(((i) & 1) * 2048), "i"(((i) & 1) * 2048 + 1024) : "memory")
        HLOAD(0); HLOAD(1); HLOAD(2); HLOAD(3); HLOAD(4); HLOAD(5); HLOAD(6);
        f32x16 acc[4];
        WdF wl[2];
#define G2STEP(i) do { \
            if ((i) >= 13 && (i) < 27) wl[((i) + 1) & 1] = wdread<((i) >= 13 && (i) < 27 ? (i) - 13 : 0)>(ta0, ta1); \
            asm volatile("s_waitcnt vmcnt(%2)" : "+v"(hf[(i) % HW][0]), "+v"(hf[(i) % HW][1]) : "i"(2 * ((i) + HW <= 28 ? HW - 1 : 27 - (i))) : "memory"); \
            const WdF& w_ = (i) < 14 ? wd[(i) < 14 ? (i) : 0] : wl[(i) & 1]; \
            if ((i) == 0) { MF32Z(acc[0], w_.a, hf[0][0]); MF32Z(acc[1], w_.a, hf[0][1]); MF32Z(acc[2], w_.b, hf[0][0]); MF32Z(acc[3], w_.b, hf[0][1]); } \
            else { MF32(acc[0], w_.a, hf[(i) % HW][0]); MF32(acc[1], w_.a, hf[(i) % HW][1]); MF32(acc[2], w_.b, hf[(i) % HW][0]); MF32(acc[3], w_.b, hf[(i) % HW][1]); } \
            if ((i) + HW < 28) HLOAD((i) + HW < 28 ? (i) + HW : 0); \
        } while (0)
        G2STEP(0);
        STAMP(6);
        G2STEP(1); G2STEP(2); G2STEP(3); G2STEP(4); G2STEP(5); G2STEP(6); G2STEP(7); G2STEP(8); G2STEP(9);
        G2STEP(10); G2STEP(11); G2STEP(12); G2STEP(13); G2STEP(14); G2STEP(15); G2STEP(16); G2STEP(17); G2STEP(18);
        G2STEP(19); G2STEP(20); G2STEP(21); G2STEP(22); G2STEP(23); G2STEP(24); G2STEP(25); G2STEP(26); G2STEP(27);
        asm volatile("s_nop 11" : "+v"(acc[0]), "+v"(acc[1]), "+v"(acc[2]), "+v"(acc[3]));
        STAMP(7);
        {
            const u32 ib = w4 * 4096 + (r >> 4) * 16384 + (r & 15) * 256;
#pragma unroll
            for (int t = 0; t < 4; ++t)
#pragma unroll
                for (int g = 0; g < 4; ++g)
                    ldsw<f32x4>(ib + (t & 1) * 32768 + (((8 * (t >> 1) + 2 * g + h) ^ (r & 7)) << 4), f32x4{acc[t][4 * g], acc[t][4 * g + 1], acc[t][4 * g + 2], acc[t][4 * g + 3]});
        }
    } else {
        const int ch = w4 >> 1, kh = w4 & 1, d = 2 * cg + ch;
        u32x2 tag;
        u32 pA, pB, pR, pI, pdt, pds;
        {
            const u32 po = (d * 256 + 128 * kh + 2 * lane) * 2, pd = d * 2;
            asm volatile("global_load_ushort %0, %6, %8\n\tglobal_load_dword %1, %7, %9\n\tglobal_load_dword %2, %7, %10\n\t"
                         "global_load_dword %3, %7, %11\n\tglobal_load_dword %4, %7, %12\n\tglobal_load_ushort %5, %6, %13"
                         : "=&v"(pdt), "=&v"(pA), "=&v"(pB), "=&v"(pR), "=&v"(pI), "=&v"(pds)
                         : "v"(pd), "v"(po), "s"(P.logdt), "s"(P.logA), "s"(P.Aim), "s"(P.Cr), "s"(P.Ci), "s"(P.Dsk) : "memory");
        }
        ld8c(tag, 0, rWS, WS_EPOCH);
        STREAM_SETUP();
        issueW(0); issueX(1);
        if (tid == 256) ldsw<u32x4>(M_CNT, u32x4{0, 0, 0, 0});
        asm volatile("s_waitcnt vmcnt(8)" : "+v"(tag), "+v"(pdt), "+v"(pA), "+v"(pB), "+v"(pR), "+v"(pI), "+v"(pds) :: "memory");
        tag = nexttag(tag);
        const float L2E = 1.44269504088896f, I2P = 0.15915494309189535f;
        const float dt = rbf(exp_acc(bflo(pdt)));
        Mul2 w1, w8, w256; Cx2 cur;
        {
            const float a0 = -rbf(exp_acc(bflo(pA))) * dt * L2E, a1 = -rbf(exp_acc(bfhi(pA))) * dt * L2E;
            const float b0 = bflo(pB) * dt * I2P, b1 = bfhi(pB) * dt * I2P;
            cexp1(a0, b0, 8.f, w8.r0, w8.n0); cexp1(a1, b1, 8.f, w8.r1, w8.n1);
            cexp1(a0, b0, 1.f, w1.r0, w1.i0); cexp1(a1, b1, 1.f, w1.r1, w1.i1);
            cexp1(a0, b0, 256.f, w256.r0, w256.i0); cexp1(a1, b1, 256.f, w256.r1, w256.i1);
            w1.n0 = -w1.i0; w1.n1 = -w1.i1; w8.i0 = -w8.n0; w8.i1 = -w8.n1; w256.n0 = -w256.i0; w256.n1 = -w256.i1;
        }
        const float dt2 = dt + dt;
        Cx2 hc0 = {bflo(pR) * dt2, bflo(pI) * dt2, bfhi(pR) * dt2, bfhi(pI) * dt2};
        asm volatile("" : "+v"(hc0.r0), "+v"(hc0.i0), "+v"(hc0.r1), "+v"(hc0.i1), "+v"(w1.r0), "+v"(w1.i0), "+v"(w1.n0), "+v"(w1.r1), "+v"(w1.i1), "+v"(w1.n1),
                     "+v"(w8.r0), "+v"(w8.i0), "+v"(w8.n0), "+v"(w8.r1), "+v"(w8.i1), "+v"(w8.n1),
                     "+v"(w256.r0), "+v"(w256.i0), "+v"(w256.n0), "+v"(w256.r1), "+v"(w256.i1), "+v"(w256.n1));
        u32x2 L[32], H[22];
#define LO(b) do { cur = cmul2(cur, w8); L[b] = packrow(cur); } while (0)
#define HI(dst, src, w, row) do { dst = cmul2(src, w); H[row] = packrow(dst); } while (0)
        L[0] = u32x2{0x00003f80u, 0x00003f80u};
        cur = Cx2{w8.r0, w8.i0, w8.r1, w8.i1}; L[1] = packrow(cur);
        STAMP(1);
        TS(0); WAITV(4); TS(1); BAR(); TS(2);
        issueW(2);
        LO(2); LO(3);
        TS(3); TS(4); BAR(); TS(5);
        issueW(3);
        LO(4); LO(5); LO(6); LO(7);
        TS(6); WAITV(4); TS(7); BAR(); TS(8);
        LO(8); LO(9); LO(10); LO(11); LO(12); LO(13); LO(14);
        TS(9); WAITV(0); TS(10); BAR(); TS(11);
        LO(15); LO(16); LO(17); LO(18); LO(19); LO(20); LO(21);
        TS(12); TS(13); BAR(); TS(14);
        LO(22); LO(23); LO(24); LO(25); LO(26); LO(27); LO(28);
        TS(15); TS(16); BAR(); TS(17);
        Cx2 h2, h4, h6;
        LO(29); LO(30); LO(31);
        H[0] = packrow(hc0);
        HI(cur, hc0, w1, 1); HI(h2, cur, w1, 2); HI(cur, h2, w1, 3);
        TS(18); TS(19); BAR(); TS(20);
        HI(h4, cur, w1, 4); HI(cur, h4, w1, 5); HI(h6, cur, w1, 6); HI(cur, h6, w1, 7); HI(hc0, hc0, w256, 8); HI(h2, h2, w256, 9);
        TS(21); TS(22); BAR(); TS(23);
        HI(h4, h4, w256, 10); HI(h6, h6, w256, 11); HI(hc0, hc0, w256, 12); HI(h4, h4, w256, 13); HI(hc0, hc0, w256, 14); HI(h4, h4, w256, 15);
        TS(24); TS(25); BAR(); TS(26);
        u32x4 gr; u32x2 fx;
        const u32 grv = w4 < 2 ? (u32)(128 * fj + 64 * w4 + lane) * 16 + (WS_CS - WS_ST) : (u32)(64 * mi + lane) * 16;
        ld16c(gr, grv, rWS, WS_ST);
        ld8c(fx, (64 * w4 + lane) * 8, rWS, WS_FX);
        HI(hc0, hc0, w256, 16); HI(hc0, hc0, w256, 17); HI(hc0, hc0, w256, 18); HI(hc0, hc0, w256, 19); HI(hc0, hc0, w256, 20);
        TS(27); TS(28); BAR(); TS(29);
        asm volatile("s_waitcnt vmcnt(0)" : "+v"(gr), "+v"(fx) :: "memory");
        waitflags(fx, tag, (64 * w4 + lane) * 8, 64, lane, rWS, WS_FX);
        {
#pragma unroll 1
            for (int it = 0; it < SPIN_MAX; ++it) {
                if (__all(gr[1] == tag.x && gr[3] == tag.x)) break;
                __builtin_amdgcn_s_sleep(1);
                ld16c(gr, grv, rWS, WS_ST); WAITV(0); PIN(gr);
            }
        }
        TS(30); TS(31); BAR(); TS(32);
        u32x4 ua, ubb;
        {
            const u32 v = (u32)(64 * w4 + (lane >> 1)) * 7168 + (2 * cg + (lane & 1)) * 16;
            ld16c(ua, v, rWS, WS_XNT); ld16c(ubb, v, rWS, WS_XNT + 32 * 7168);
        }
        H[21] = u32x2{0, 0};
        const u32 stg = w4 < 2 ? STG01 + w4 * 8192 : STG23 + (w4 - 2) * 8192;
        u32 sw0 = stg + lane * 16, sw1 = stg + (lane ^ 1) * 16, sw2 = stg + (lane ^ 4) * 16, sw3 = stg + (lane ^ 5) * 16;
        u32 sw4 = stg + (lane ^ 8) * 16, sw5 = stg + (lane ^ 9) * 16, sw6 = stg + (lane ^ 12) * 16, sw7 = stg + (lane ^ 13) * 16;
        u32 sr[4];
        {
            const int r = lane & 15, q = lane >> 4, pp = r >> 1, g = (pp & 1) | ((pp & 6) << 1);
#pragma unroll
            for (int i = 0; i < 4; ++i) sr[i] = stg + pp * 1024 + (((8 * (i >> 1) + 2 * q + (i & 1)) ^ g) << 4) + (r & 1) * 8;
        }
        asm volatile("" : "+v"(sr[0]), "+v"(sr[1]), "+v"(sr[2]), "+v"(sr[3]));
#define PUT(p, A, i) (*(volatile u32x4 LAS*)(uintptr_t)(sw##p + (p) * 1024) = u32x4{A[i].x, A[i].y, A[(i) + 1].x, A[(i) + 1].y})
        auto getf = [&](int k) {
            const u32x2 a = *(const volatile u32x2 LAS*)(uintptr_t)(sr[2 * (k & 1)] + (k >> 1) * 256), b = *(const volatile u32x2 LAS*)(uintptr_t)(sr[2 * (k & 1) + 1] + (k >> 1) * 256);
            return u32x4{a.x, a.y, b.x, b.y};
        };
        if (w4 < 2) { ldsw<u32>(M_CSG + (64 * w4 + lane) * 4, gr[0]); ldsw<u32>(M_CSU + (64 * w4 + lane) * 4, gr[2]); }
        else if (w4 == 2) {
            const u32 n0 = (gr[0] & 0xffffu) ^ 0x8000u, n1 = (gr[2] & 0xffffu) ^ 0x8000u;
            ldsw<u32x2>(M_NM + lane * 8, u32x2{n0 | (n0 << 16), n1 | (n1 << 16)});
            const float r0 = __builtin_amdgcn_rcpf(bfhi(gr[0])), r1 = __builtin_amdgcn_rcpf(bfhi(gr[2]));
            ldsw<u32>(M_RS2 + lane * 8, __float_as_uint(r0 * r0)); ldsw<u32>(M_RS2 + lane * 8 + 4, __float_as_uint(r1 * r1));
        } else {
            ldsw<u32x4>(KN + (lane >> 5) * 4608 + (lane & 31) * 16, u32x4{0, 0, 0, 0});
            if (lane < 16) ldsw<u32x4>(UT + (lane >> 2) * 4160 + 4096 + (lane & 3) * 16, u32x4{0, 0, 0, 0});
        }
        u32x4 lo[2][8];
        STAMP(9);
        PUT(0, L, 0); PUT(1, L, 2); PUT(2, L, 4); PUT(3, L, 6);
        TS(33); TS(34); BAR(); TS(35);
        PUT(4, L, 8); PUT(5, L, 10); PUT(6, L, 12); PUT(7, L, 14);
#pragma unroll
        for (int k = 0; k < 8; ++k) lo[0][k] = getf(k);
        asm volatile("s_waitcnt vmcnt(0)" : "+v"(ua), "+v"(ubb) :: "memory");
        {
            const u32 au = UT + (lane & 1) * 8320 + (255 - (64 * w4 + (lane >> 1))) * 16;
            ldsw<u32x4>(au, u32x4{__builtin_amdgcn_alignbit(ua[3], ua[3], 16), __builtin_amdgcn_alignbit(ua[2], ua[2], 16),
                                  __builtin_amdgcn_alignbit(ua[1], ua[1], 16), __builtin_amdgcn_alignbit(ua[0], ua[0], 16)});
            ldsw<u32x4>(au - 512, u32x4{__builtin_amdgcn_alignbit(ubb[3], ubb[3], 16), __builtin_amdgcn_alignbit(ubb[2], ubb[2], 16),
                                        __builtin_amdgcn_alignbit(ubb[1], ubb[1], 16), __builtin_amdgcn_alignbit(ubb[0], ubb[0], 16)});
        }
        TS(36); TS(37); BAR(); TS(38);
        const u32 ut = UT + ch * 8320, kn = KN + ch * 4608;
        const u32 ac1 = ut + (64 * kh + lane) * 16;
        const u32x4 E0 = ldsr<u32x4>(ac1), E1 = ldsr<u32x4>(ac1 + 2048); const u32 F0 = ldsr<u32>(ac1 + 16), F1 = ldsr<u32>(ac1 + 2064);
        PUT(0, L, 16); PUT(1, L, 18); PUT(2, L, 20); PUT(3, L, 22); PUT(4, L, 24); PUT(5, L, 26); PUT(6, L, 28); PUT(7, L, 30);
        TS(39); TS(40); BAR(); TS(41);
#pragma unroll
        for (int k = 0; k < 8; ++k) lo[1][k] = getf(k);
        PUT(0, H, 0); PUT(1, H, 2); PUT(2, H, 4); PUT(3, H, 6); PUT(4, H, 8); PUT(5, H, 10);
        TS(42); TS(43); BAR(); TS(44);
        STAMP(2);
        PUT(6, H, 12); PUT(7, H, 14);
        f32x4 kacc[2][2];
        {
            u32x4 a[8], b[8];
#pragma unroll
            for (int k = 0; k < 8; ++k) a[k] = getf(k);
            PUT(0, H, 16); PUT(1, H, 18); PUT(2, H, 20);
#pragma unroll
            for (int k = 0; k < 8; ++k) b[k] = getf(k);
            ldsw<u32x4>(ac1 + 4160, u32x4{__builtin_amdgcn_alignbit(E0[1], E0[0], 16), __builtin_amdgcn_alignbit(E0[2], E0[1], 16),
                                          __builtin_amdgcn_alignbit(E0[3], E0[2], 16), __builtin_amdgcn_alignbit(F0, E0[3], 16)});
            ldsw<u32x4>(ac1 + 2048 + 4160, u32x4{__builtin_amdgcn_alignbit(E1[1], E1[0], 16), __builtin_amdgcn_alignbit(E1[2], E1[1], 16),
                                                 __builtin_amdgcn_alignbit(E1[3], E1[2], 16), __builtin_amdgcn_alignbit(F1, E1[3], 16)});
            const f32x4 zz = {0.f, 0.f, 0.f, 0.f};
            kacc[0][0] = zz; kacc[0][1] = zz; kacc[1][0] = zz; kacc[1][1] = zz;
#pragma unroll
            for (int k = 0; k < 8; ++k) { kacc[0][0] = mfma16(a[k], lo[0][k], kacc[0][0]); kacc[0][1] = mfma16(a[k], lo[1][k], kacc[0][1]); }
#pragma unroll
            for (int k = 0; k < 8; ++k) { kacc[1][0] = mfma16(lo[0][k], b[k], kacc[1][0]); kacc[1][1] = mfma16(lo[1][k], b[k], kacc[1][1]); }
        }
        {
            const int b = lane & 15, q = lane >> 4;
            const int sh = q < 2 ? 5 : 6 - q, bs = q < 2 ? 16 * q : 512 * q;
            const u32 aA = stg + (b << sh) + bs, aB = aA + (q == 3 ? 256 : 8), st = 16 << sh;
            ldsw<u32x2>(aA, u32x2{__float_as_uint(kacc[0][0][0]), __float_as_uint(kacc[0][0][1])}); ldsw<u32x2>(aB, u32x2{__float_as_uint(kacc[0][0][2]), __float_as_uint(kacc[0][0][3])});
            ldsw<u32x2>(aA + st, u32x2{__float_as_uint(kacc[0][1][0]), __float_as_uint(kacc[0][1][1])}); ldsw<u32x2>(aB + st, u32x2{__float_as_uint(kacc[0][1][2]), __float_as_uint(kacc[0][1][3])});
            const u32 aC = stg + (512 + 32 * min(b, 5) + 4 * q) * 4;
            ldsw<f32x4>(aC, kacc[1][0]); ldsw<f32x4>(aC + 64, kacc[1][1]);
        }
        STAMP(10);
        lds_arrive(M_CNT, lane); lds_wait(M_CNT, 4);
        {
            const u32 pa = stg - kh * 8192;
            const u32 a3 = pa + (512 + 64 * kh + lane) * 4;
            const float D0 = ldsr<float>(a3) + ldsr<float>(a3 + 8192), D1 = ldsr<float>(a3 + 4) + ldsr<float>(a3 + 8196);
            u32x4 o;
            if (kh == 0) {
                const bool s0 = lane < 32;
                const u32 a = pa + (s0 ? 8 * lane : 128 + 4 * lane) * 4;
                const f32x4 A = ldsr<f32x4>(a) + ldsr<f32x4>(a + 8192), B = ldsr<f32x4>(a + 16) + ldsr<f32x4>(a + 8208);
                o[0] = pkbf(A[0], s0 ? A[1] : A[0]);
                o[1] = pkbf(s0 ? A[2] : A[1], s0 ? A[3] : A[2]);
                o[2] = pkbf(s0 ? B[0] : A[2], s0 ? B[1] : A[2]);
                o[3] = pkbf(s0 ? B[2] : A[3], s0 ? B[3] : B[0]);
            } else {
                const u32 a = pa + (384 + 2 * lane) * 4;
                const float A0 = ldsr<float>(a) + ldsr<float>(a + 8192), A1 = ldsr<float>(a + 4) + ldsr<float>(a + 8196), A2 = ldsr<float>(a + 8) + ldsr<float>(a + 8200);
                o = u32x4{pkbf(A0, A0), pkbf(A0, A1), pkbf(A1, A1), pkbf(A2, A2)};
            }
            const u32 ao = kn + 512 + (64 * kh + lane) * 16;
            ldsw<u32x4>(ao, o);
            const u32 d00 = pkbf(D0, D0);
            ldsw<u32x4>(ao + 2048, u32x4{d00, d00, pkbf((lane & 1) ? D1 : D0, D1), pkbf(D1, D1)});
        }
        lds_arrive(M_CNT, lane); lds_wait(M_CNT, 8);
        STAMP(3);
        f32x4 y[8];
        {
            const int r = lane & 15, q = lane >> 4, dl = q >> 1, q1 = q & 1;
            u32 va = ut + (1 - (r & 1)) * 4160 + 2 * (2046 - 16 * dl - (r & 14) + 8 * q1 - 128 * kh) - 192 - 3584;
            u32 vb = kn + 512 + 32 * (r - dl) + 16 * q1 - 256 * kh - 192;
            u32 vq[4];
#pragma unroll
            for (int i = 0; i < 4; ++i) vq[i] = va + 1024 * i;
            asm volatile("" : "+v"(vq[0]), "+v"(vq[1]), "+v"(vq[2]), "+v"(vq[3]), "+v"(vb));
            u32x4 A[2][8], B[2][8];
            auto rd = [&](int k, int n, u32x4 (&a)[8], u32x4 (&b)[8]) {
                if (n >= 2 && n < 10) b[n - 2] = *(const volatile u32x4 LAS*)(uintptr_t)(vb + 64 * (3 - k) + 512 * (n - 2));
                else {
                    const int i = n < 2 ? 0 : (n - 8) >> 1;
                    typedef __attribute__((ext_vector_type(2), aligned(4))) u32 u32x2a;
                    const u32x2 p = *(const volatile u32x2a LAS*)(uintptr_t)(vq[(7 - i) >> 1] + 64 * (3 - k) + 512 * ((7 - i) & 1) + 8 * (n & 1));
                    if (n & 1) { a[i][2] = p.x; a[i][3] = p.y; } else { a[i][0] = p.x; a[i][1] = p.y; }
                }
            };
#pragma unroll
            for (int n = 0; n < 24; ++n) rd(0, n, A[0], B[0]);
#pragma unroll
            for (int k = 0; k < 4; ++k) {
                int n = 0;
#pragma unroll
                for (int i = 0; i < 8; ++i)
#pragma unroll
                    for (int t = i; t < 8; ++t) {
                        if (k == 0 && i == 0) asm volatile("v_mfma_f32_16x16x32_bf16 %0, %1, %2, 0" : "=&v"(y[t]) : "v"(A[0][0]), "v"(B[0][t]));
                        else asm volatile("v_mfma_f32_16x16x32_bf16 %0, %1, %2, %0" : "+v"(y[t]) : "v"(A[k & 1][i]), "v"(B[k & 1][t - i]));
                        if (k < 3 && n < 24) rd(k + 1, n, A[(k + 1) & 1], B[(k + 1) & 1]);
                        ++n;
                    }
            }
            asm volatile("s_nop 7" : "+v"(y[0]), "+v"(y[1]), "+v"(y[2]), "+v"(y[3]), "+v"(y[4]), "+v"(y[5]), "+v"(y[6]), "+v"(y[7]));
        }
        STAMP(4);
        u32x2 uu[4];
        {
            const u32 e = EXC + w4 * 4096 + lane * 16;
            if (kh) {
#pragma unroll
                for (int t = 0; t < 4; ++t) ldsw<f32x4>(e + t * 1024, y[t]);
#pragma unroll
                for (int t = 0; t < 4; ++t) y[t] = y[4 + t];
            } else {
#pragma unroll
                for (int t = 0; t < 4; ++t) ldsw<f32x4>(e + t * 1024, y[4 + t]);
            }
            const u32 a = ut + (2044 - 768 - (1024 * kh + 16 * (lane & 15) + 4 * (lane >> 4))) * 2;
#pragma unroll
            for (int t = 0; t < 4; ++t) uu[t] = ldsr<u32x2>(a + (3 - t) * 512);
        }
        lds_arrive(M_CNT + 4, lane);
        {
            const float ds = bflo(pds);
#pragma unroll
            for (int t = 0; t < 4; ++t) {
                y[t][0] = __builtin_fmaf(bfhi(uu[t].y), ds, y[t][0]); y[t][1] = __builtin_fmaf(bflo(uu[t].y), ds, y[t][1]);
                y[t][2] = __builtin_fmaf(bfhi(uu[t].x), ds, y[t][2]); y[t][3] = __builtin_fmaf(bflo(uu[t].x), ds, y[t][3]);
            }
        }
        lds_wait(M_CNT + 4, 4);
        {
            const u32 e = EXC + (w4 ^ 1) * 4096 + lane * 16;
            u32x2 z[4];
#pragma unroll
            for (int t = 0; t < 4; ++t) {
                const f32x4 v = ldsr<f32x4>(e + t * 1024);
                z[t] = u32x2{pkbf(addf(v[0], y[t][0]), addf(v[1], y[t][1])), pkbf(addf(v[2], y[t][2]), addf(v[3], y[t][3]))};
            }
            const int q = lane >> 4;
#pragma unroll
            for (int t = 0; t < 4; t += 2) {
                auto a = __builtin_amdgcn_permlane16_swap(z[t].x, z[t + 1].x, false, false);
                auto b = __builtin_amdgcn_permlane16_swap(z[t].y, z[t + 1].y, false, false);
                st16c(u32x4{a[0], b[0], a[1], b[1]}, (u32)(1024 * kh + 256 * (t + (q & 1)) + 16 * (lane & 15) + 8 * (q >> 1)) * 2, rWS, WS_ZT + d * 4096);
            }
        }
        asm volatile("buffer_load_dwordx4 %0, %1, %2, 0 offen" : "=v"(xres) : "v"((u32)(64 * mt + (tid >> 3)) * 896 + nj * 128 + (tid & 7) * 16), "s"(rX) : "memory");
        asm volatile("s_waitcnt vmcnt(0)" : "+v"(xres) :: "memory");
        if (lane == 0) stflag(P.ws + WS_FZ + (2 * d + kh) * 8, tag);
        STAMP(5);
        {
            const u32 fv = (u32)(2 * (64 * nj + 16 * w4 + (lane & 15)) + (mt >> 4)) * 8;
            u32x2 fz;
            ld8c(fz, fv, rWS, WS_FZ);
            asm volatile("s_waitcnt vmcnt(0)" : "+v"(fz) :: "memory");
            waitflags(fz, tag, fv, 64, lane, rWS, WS_FZ);
            const u32 v = (u32)(64 * nj + 16 * w4 + (lane >> 3)) * 4096 + 128 * mt + (lane & 7) * 16;
            dma1c(ZTL + (2 * w4) * 1040, v, rWS, WS_ZT);
            dma1c(ZTL + (2 * w4 + 1) * 1040, v, rWS, WS_ZT + 8 * 4096);
            WAITV(0);
        }
        STAMP(6);
    }
    BAR();
    {
        const int l = tid >> 3, dc = tid & 7;
        const u32 i0 = (l >> 4) * 16384 + (l & 15) * 256 + (((2 * dc) ^ (l & 7)) << 4), i1 = i0 ^ 16;
        f32x4 a = ldsr<f32x4>(i0), b = ldsr<f32x4>(i1);
#pragma unroll
        for (int w = 1; w < 4; ++w) { a += ldsr<f32x4>(i0 + w * 4096); b += ldsr<f32x4>(i1 + w * 4096); }
        const float rs2 = ldsr<float>(M_RS2 + (64 * (xcd & 1) + l) * 4);
        float o[8];
#pragma unroll
        for (int e = 0; e < 8; ++e) {
            const float z = bflo(ldsr<u16>(ZTL + dc * 1040 + e * 128 + l * 2));
            const float xr = (e & 1) ? bfhi(xres[e >> 1]) : bflo(xres[e >> 1]);
            o[e] = __builtin_fmaf(e < 4 ? a[e & 3] : b[e & 3], rs2, xr + z);
        }
        *(u32x4*)(P.out + (size_t)(64 * mt + l) * 896 + nj * 128 + dc * 16) = u32x4{pkbf(o[0], o[1]), pkbf(o[2], o[3]), pkbf(o[4], o[5]), pkbf(o[6], o[7])};
    }
    STAMP(8); CSTAMP(15); TSDUMP();
}

extern "C" void kernel_launch(void* const* d_in, const int* in_sizes, int n_in, void* d_out, int out_size, void* d_ws, size_t ws_size,
                              hipStream_t stream) {
    Params P;
    P.x = (const char*)d_in[0]; P.logA = (const char*)d_in[1]; P.Aim = (const char*)d_in[2]; P.Cr = (const char*)d_in[3];
    P.Ci = (const char*)d_in[4]; P.logdt = (const char*)d_in[5]; P.Dsk = (const char*)d_in[6];
    P.Wg = (const char*)d_in[8]; P.Wu = (const char*)d_in[9]; P.Wd = (const char*)d_in[10];
    P.out = (char*)d_out; P.ws = (char*)d_ws;
    (void)hipFuncSetAttribute((const void*)fused_kernel, hipFuncAttributeMaxDynamicSharedMemorySize, LDS_BYTES);
    hipLaunchKernelGGL(fused_kernel, dim3(256), dim3(512), LDS_BYTES, stream, P);
}
```

```cpp
#include <hip/hip_runtime.h>
#include <stdint.h>

typedef unsigned u32;
typedef unsigned long long u64;
typedef unsigned short u16;
typedef __attribute__((ext_vector_type(2))) u32 u32x2;
typedef __attribute__((ext_vector_type(4))) u32 u32x4;
typedef __attribute__((ext_vector_type(4))) float f32x4;
typedef __attribute__((ext_vector_type(16))) float f32x16;
typedef __attribute__((ext_vector_type(8))) __bf16 bf16x8;
typedef __attribute__((ext_vector_type(2))) __bf16 bf16x2;
typedef __attribute__((ext_vector_type(4))) short s16x4;
typedef __amdgpu_buffer_rsrc_t rsrc_t;
#define LAS __attribute__((address_space(3)))
#define GAS __attribute__((address_space(1)))
#define DI __device__ __forceinline__

constexpr u32 WS_EPOCH = 0, WS_FX = 256, WS_FZ = 2560, WS_FH = 10240, WS_ST = 18432, WS_CS = 34816;
constexpr u32 WS_XNT = 65536, WS_ZT = WS_XNT + 1835008, WS_H = WS_ZT + 1835008, WS_END = WS_H + 7340032;
constexpr u32 WS_DBG = 16u << 20;
constexpr u32 XS0 = 0, WS0 = 49152, SSM = 114688;
constexpr u32 STG = SSM;
constexpr u32 KC = SSM;
constexpr u32 UB = SSM + 33280;
constexpr u32 MISC = SSM + 42496;
constexpr u32 M_NM = MISC, M_RS2 = MISC + 512, M_CSG = MISC + 1024, M_CSU = MISC + 1536, M_CNT = MISC + 2048;
constexpr u32 EXC = SSM + 16640;
constexpr u32 ZTL = SSM;
constexpr u32 LDS_BYTES = 163840;
constexpr int SPIN_MAX = 1 << 18;
#define ROT1 ((mi + fj) % 7)
#define EARLYD 0

#define TS(i) do {} while (0)
#define TSDUMP() do {} while (0)
#define STAMP(i) do {} while (0)
#define CSTAMP(i) do {} while (0)

struct Params {
    const char* x; const char* logA; const char* Aim; const char* Cr; const char* Ci; const char* logdt; const char* Dsk;
    const char* Wg; const char* Wu; const char* Wd; char* out; char* ws;
};

DI float bflo(u32 v) { return __uint_as_float(v << 16); }
DI float bfhi(u32 v) { return __uint_as_float(v & 0xffff0000u); }
DI u32 pkbf(float a, float b) { bf16x2 v = {(__bf16)a, (__bf16)b}; return __builtin_bit_cast(u32, v); }
DI float rbf(float a) { return bflo(pkbf(a, 0.f)); }
DI bf16x8 asbf(u32x4 v) { return __builtin_bit_cast(bf16x8, v); }
template <class T> DI T ldsr(u32 a) { return *(const T LAS*)(uintptr_t)a; }
template <class T> DI void ldsw(u32 a, T v) { *(T LAS*)(uintptr_t)a = v; }
DI u32x2 ldstr(u32 a) { return __builtin_bit_cast(u32x2, __builtin_amdgcn_ds_read_tr16_b64_v4i16((s16x4 LAS*)(uintptr_t)a)); }
DI u32x4 ldstr2(u32 a, u32 b) { u32x2 p = ldstr(a), q = ldstr(b); return u32x4{p.x, p.y, q.x, q.y}; }
#define WAITV(n) asm volatile("s_waitcnt vmcnt(" #n ")" ::: "memory")
#define BAR() asm volatile("s_waitcnt lgkmcnt(0)\n\ts_barrier" ::: "memory")
#define SB() __builtin_amdgcn_sched_barrier(0)

DI void dma4(u32 m0v, u32 v0, u32 v1, u32 v2, u32 v3, rsrc_t r, u32 soff) {
    asm volatile("s_mov_b32 m0, %0\n\ts_nop 0\n\t"
                 "buffer_load_dwordx4 %1, %5, %6 offen lds\n\t"
                 "buffer_load_dwordx4 %2, %5, %6 offen offset:1024 lds\n\t"
                 "buffer_load_dwordx4 %3, %5, %6 offen offset:2048 lds\n\t"
                 "buffer_load_dwordx4 %4, %5, %6 offen offset:3072 lds"
                 :: "s"(m0v), "v"(v0), "v"(v1), "v"(v2), "v"(v3), "s"(r), "s"(soff) : "memory");
}
DI void dma1c(u32 m0v, u32 v, rsrc_t r, u32 soff) {
    asm volatile("s_mov_b32 m0, %0\n\ts_nop 0\n\t"
                 "buffer_load_dwordx4 %1, %2, %3 offen sc1 lds"
                 :: "s"(m0v), "v"(v), "s"(r), "s"(soff) : "memory");
}
DI void ld16c(u32x4& d, u32 v, rsrc_t r, u32 soff) { asm volatile("buffer_load_dwordx4 %0, %1, %2, %3 offen sc1" : "=v"(d) : "v"(v), "s"(r), "s"(soff) : "memory"); }
DI void ld8c(u32x2& d, u32 v, rsrc_t r, u32 soff) { asm volatile("buffer_load_dwordx2 %0, %1, %2, %3 offen sc1" : "=v"(d) : "v"(v), "s"(r), "s"(soff) : "memory"); }
#define PIN(x) asm volatile("" : "+v"(x) :: "memory")
DI void st16c(u32x4 d, u32 v, rsrc_t r, u32 off) { __builtin_amdgcn_raw_buffer_store_b128(d, r, v + off, 0, 16); }
DI void stflag(char* p, u32x2 tag) { __hip_atomic_store((GAS u64*)(uintptr_t)p, ((u64)tag.y << 32) | tag.x, __ATOMIC_RELAXED, __HIP_MEMORY_SCOPE_AGENT); }
DI u32x2 nexttag(u32x2 e) { u64 v = (((u64)e.y << 32) | e.x) + 1; return u32x2{(u32)v, (u32)(v >> 32)}; }
DI void waitflags(u32x2 f, u32x2 tag, u32 v, int n, int lane, rsrc_t r, u32 soff) {
#pragma unroll 1
    for (int it = 0; it < SPIN_MAX; ++it) {
        bool ok = (lane >= n) || (f.x == tag.x && f.y == tag.y);
        if (__all(ok)) break;
        __builtin_amdgcn_s_sleep(1);
        ld8c(f, v, r, soff); WAITV(0); PIN(f);
    }
}
DI void lds_arrive(u32 a, int lane) {
    asm volatile("s_waitcnt lgkmcnt(0)" ::: "memory");
    if (lane == 0) __hip_atomic_fetch_add((LAS u32*)(uintptr_t)a, 1u, __ATOMIC_RELAXED, __HIP_MEMORY_SCOPE_WORKGROUP);
}
DI void lds_wait(u32 a, u32 target) {
#pragma unroll 1
    for (int it = 0; it < SPIN_MAX; ++it) {
        u32 c = __hip_atomic_load((LAS u32*)(uintptr_t)a, __ATOMIC_RELAXED, __HIP_MEMORY_SCOPE_WORKGROUP);
        if (__builtin_amdgcn_readfirstlane(c) >= target) break;
    }
    asm volatile("" ::: "memory");
}
DI f32x16 mfma32(u32x4 a, u32x4 b, f32x16 c) { return __builtin_amdgcn_mfma_f32_32x32x16_bf16(asbf(a), asbf(b), c, 0, 0, 0); }
DI f32x4 mfma16(u32x4 a, u32x4 b, f32x4 c) { return __builtin_amdgcn_mfma_f32_16x16x32_bf16(asbf(a), asbf(b), c, 0, 0, 0); }
DI f32x4 sel4(int c, f32x4 a, f32x4 b) { asm("" : "+v"(a), "+v"(b)); return c ? a : b; }
DI float dot2(u32 a, u32 b, float c) { return __builtin_amdgcn_fdot2_f32_bf16(__builtin_bit_cast(bf16x2, a), __builtin_bit_cast(bf16x2, b), c, false); }
template <int C> DI float dppf(float v) { return __int_as_float(__builtin_amdgcn_mov_dpp(__float_as_int(v), C, 0xf, 0xf, true)); }
DI float exp_acc(float x) {
    const float L = 1.44269502162933349609375f, Ll = 1.92596299112661746e-8f;
    float hi = x * L, lo = __builtin_fmaf(x, L, -hi) + x * Ll;
    float e = __builtin_amdgcn_exp2f(hi);
    return __builtin_fmaf(e * lo, 0.693147182464599609375f, e);
}

struct Cx2 { float r0, i0, r1, i1; };
struct Mul2 { float r0, i0, n0, r1, i1, n1; };
DI Cx2 cmul2(const Cx2& a, const Mul2& w) {
    Cx2 t;
    asm("v_mul_f32 %0, %4, %8\n\tv_mul_f32 %1, %4, %9\n\tv_mul_f32 %2, %6, %11\n\tv_mul_f32 %3, %6, %12\n\t"
        "v_fmac_f32 %0, %5, %10\n\tv_fmac_f32 %1, %5, %8\n\tv_fmac_f32 %2, %7, %13\n\tv_fmac_f32 %3, %7, %11"
        : "=&v"(t.r0), "=&v"(t.i0), "=&v"(t.r1), "=&v"(t.i1)
        : "v"(a.r0), "v"(a.i0), "v"(a.r1), "v"(a.i1), "v"(w.r0), "v"(w.i0), "v"(w.n0), "v"(w.r1), "v"(w.i1), "v"(w.n1));
    return t;
}
struct Unit { u32 m0, so; };
template <int I> DI void piece(const Unit& u, u32 v, rsrc_t r) {
    asm volatile("s_mov_b32 m0, %0\n\ts_nop 0\n\tbuffer_load_dwordx4 %1, %2, %3 offen offset:%4 lds" :: "s"(u.m0), "v"(v), "s"(r), "s"(u.so), "i"(I * 1024) : "memory");
}
DI float addf(float a, float b) { float r; asm("v_add_f32 %0, %1, %2" : "=v"(r) : "v"(a), "v"(b)); return r; }
DI u32x2 packrow(const Cx2& v) { u32x2 o = {pkbf(v.r0, v.i0), pkbf(v.r1, v.i1)}; asm volatile("" : "+v"(o)); return o; }
DI void cexp1(float a2, float br, float p, float& wr, float& wi) {
    float e = __builtin_amdgcn_exp2f(a2 * p), ph = __builtin_amdgcn_fractf(br * p);
    wr = e * __builtin_amdgcn_cosf(ph); wi = e * __builtin_amdgcn_sinf(ph);
}

DI void ln_load(u32x4& a, u32x4& b, int blk, int w4, int lane, rsrc_t rX) {
    const u32 v = lane < 56 ? (u32)(8 * blk + 2 * w4) * 896 + lane * 16 : 0x7fff0000u;
    asm volatile("buffer_load_dwordx4 %0, %2, %3, 0 offen\n\tbuffer_load_dwordx4 %1, %2, %3, 0 offen offset:896" : "=&v"(a), "=&v"(b) : "v"(v), "s"(rX) : "memory");
}
DI float red2(float s0, float s1) {
    auto r = __builtin_amdgcn_permlane32_swap(__float_as_uint(s0), __float_as_uint(s1), false, false);
    float t = __uint_as_float(r[0]) + __uint_as_float(r[1]);
    auto q = __builtin_amdgcn_permlane16_swap(__float_as_uint(t), __float_as_uint(t), false, false);
    t = __uint_as_float(q[0]) + __uint_as_float(q[1]);
    t += dppf<0xB1>(t); t += dppf<0x4E>(t); t += dppf<0x141>(t); t += dppf<0x140>(t);
    return t;
}
DI void ln_compute(u32x4 a, u32x4 b, int blk, int w4, int lane, u32 tagx, rsrc_t rWS) {
    float s0 = 0.f, s1 = 0.f;
#pragma unroll
    for (int j = 0; j < 4; ++j) { s0 = dot2(a[j], 0x3f803f80u, s0); s1 = dot2(b[j], 0x3f803f80u, s1); }
    const float mean = red2(s0, s1) * (1.f / 448.f), mb = rbf(mean);
    auto mm = __builtin_amdgcn_permlane32_swap(__float_as_uint(mb), __float_as_uint(mb), false, false);
    const float m0 = __uint_as_float(mm[0]), m1 = __uint_as_float(mm[1]);
    float q0 = 0.f, q1 = 0.f;
#pragma unroll
    for (int j = 0; j < 4; ++j) {
        float c = bflo(a[j]) - m0, d = bfhi(a[j]) - m0, e = bflo(b[j]) - m1, f = bfhi(b[j]) - m1;
        q0 = __builtin_fmaf(c, c, q0); q0 = __builtin_fmaf(d, d, q0); q1 = __builtin_fmaf(e, e, q1); q1 = __builtin_fmaf(f, f, q1);
        a[j] = pkbf(c, d); b[j] = pkbf(e, f);
    }
    if (lane >= 56) { q0 = 0.f; q1 = 0.f; }
    const float dm = mean - mb;
    const float var = rbf(fmaxf(red2(q0, q1) * (1.f / 448.f) - dm * dm, 0.f));
    const float sd = rbf(__builtin_sqrtf(rbf(var + 1e-5f)));
    auto pp = __builtin_amdgcn_permlane32_swap(pkbf(mb, sd), pkbf(mb, sd), false, false);
    if (lane == 0) st16c(u32x4{pp[0], tagx, pp[1], tagx}, (u32)(4 * blk + w4) * 16, rWS, WS_ST);
    const float r0 = __builtin_amdgcn_rcpf(bfhi(pp[0])), r1 = __builtin_amdgcn_rcpf(bfhi(pp[1]));
    u32x4 o, p;
#pragma unroll
    for (int j = 0; j < 4; ++j) { o[j] = pkbf(bflo(a[j]) * r0, bfhi(a[j]) * r0); p[j] = pkbf(bflo(b[j]) * r1, bfhi(b[j]) * r1); }
    if (lane < 56) { ldsw<u32x4>(UB + 2 * w4 * 912 + lane * 16, o); ldsw<u32x4>(UB + (2 * w4 + 1) * 912 + lane * 16, p); }
}
DI void ln_store(int blk, int w4, int lane, rsrc_t rWS) {
#pragma unroll
    for (int i = 0; i < 2; ++i) {
        const int it = i == 0 ? w4 : (w4 < 3 ? w4 + 4 : 3);
        const u32 a = UB + ((lane & 15) >> 2) * 912 + (4 * it + (lane >> 4)) * 32 + (lane & 3) * 8;
        st16c(ldstr2(a, a + 4 * 912), (u32)blk * 7168 + it * 1024 + lane * 16, rWS, WS_XNT);
    }
}

DI void helper_wg(const Params& P, int hw, int tid, int lane, int wave, rsrc_t rWS, rsrc_t rX) {
    STAMP(0);
    u32x2 tag;
    ld8c(tag, 0, rWS, WS_EPOCH);
    u32x4 ra, rb, wv[14];
    ln_load(ra, rb, blockIdx.x, wave & 3, lane, rX);
    const int c = lane & 7, hwc = hw < 28 ? hw : 0;
    const char* wp = (((tid >> 3) & 1) ? P.Wu : P.Wg) + (size_t)(tid >> 4) * 3584 + hwc * 128 + c * 16;
#pragma unroll
    for (int i = 0; i < 14; ++i) wv[i] = *(const u32x4*)(wp + (size_t)i * 32 * 3584);
    SB();
    asm volatile("s_waitcnt vmcnt(14)" : "+v"(tag), "+v"(ra), "+v"(rb) :: "memory");
    tag = nexttag(tag);
    if (wave < 4) ln_compute(ra, rb, blockIdx.x, wave, lane, tag.x, rWS);
    BAR();
    if (wave < 4) ln_store(blockIdx.x, wave, lane, rWS);
    STAMP(1);
    if (hw < 28) {
        float a[8];
        u32 c10 = 0x00003f80u;
        asm("" : "+s"(c10));
#pragma unroll
        for (int j = 0; j < 8; ++j) a[j] = 0.f;
#pragma unroll
        for (int i = 0; i < 14; ++i)
#pragma unroll
            for (int j = 0; j < 4; ++j) { a[2 * j] = dot2(wv[i][j], c10, a[2 * j]); a[2 * j + 1] = dot2(wv[i][j], 0x3f800000u, a[2 * j + 1]); }
        float s4[4], t2[2];
#pragma unroll
        for (int j = 0; j < 4; ++j) { auto r = __builtin_amdgcn_permlane32_swap(__float_as_uint(a[j]), __float_as_uint(a[j + 4]), false, false); s4[j] = __uint_as_float(r[0]) + __uint_as_float(r[1]); }
#pragma unroll
        for (int j = 0; j < 2; ++j) { auto r = __builtin_amdgcn_permlane16_swap(__float_as_uint(s4[j]), __float_as_uint(s4[j + 2]), false, false); t2[j] = __uint_as_float(r[0]) + __uint_as_float(r[1]); }
        ldsw<u32x2>(wave * 512 + ((lane >> 3) & 1) * 256 + (8 * c + 2 * ((lane >> 4) & 1) + 4 * (lane >> 5)) * 4, u32x2{__float_as_uint(t2[0]), __float_as_uint(t2[1])});
    }
    WAITV(0);
    BAR();
    STAMP(2);
    if (wave == 1 && lane == 0) stflag(P.ws + WS_FX + blockIdx.x * 8, tag);
    if (wave == 0) {
        if (hw < 28) {
            float g = 0.f, u = 0.f;
#pragma unroll
            for (int w = 0; w < 8; ++w) { g += ldsr<float>(w * 512 + lane * 4); u += ldsr<float>(w * 512 + 256 + lane * 4); }
            float gh = rbf(g), uh = rbf(u);
            st16c(u32x4{pkbf(gh, g - gh), tag.x, pkbf(uh, u - uh), tag.x}, (u32)(64 * hw + lane) * 16, rWS, WS_CS);
        } else if (hw == 31) {
            u32x2 f;
#pragma unroll 1
            for (int i = 0; i < 4; ++i) { ld8c(f, lane * 8 + i * 512, rWS, WS_FX); WAITV(0); PIN(f); waitflags(f, tag, lane * 8 + i * 512, 64, lane, rWS, WS_FX); }
            if (lane == 0) stflag(P.ws + WS_EPOCH, tag);
        }
    }
    STAMP(3);
}

#define MF32(acc, a, b) asm volatile("v_mfma_f32_32x32x16_bf16 %0, %1, %2, %0" : "+v"(acc) : "v"(a), "v"(b) : "memory")
#define MF32Z(acc, a, b) asm volatile("v_mfma_f32_32x32x16_bf16 %0, %1, %2, 0" : "=&v"(acc) : "v"(a), "v"(b) : "memory")
struct G1Frag { u32x4 g0, g1, u0, u1, x0, x1; };
template <int J, int S, int N> DI void g1rd(G1Frag& f, const u32 (&xa)[4], u32 wa0, u32 wa1) {
    constexpr u32 wo = (J & 3) * 16384 + S * 4096, xo = XS0 + ((J >> 1) % 3) * 16384;
    const u32 xv = xa[(J & 1) * 2 + S] + xo;
    if (N == 0) f.g0 = ldstr2(wa0 + wo, wa0 + wo + 1024);
    if (N == 1) { f.x0 = ldsr<u32x4>(xv); const u32x2 p = ldstr(wa0 + wo + 8192); f.u0[0] = p.x; f.u0[1] = p.y; }
    if (N == 2) { const u32x2 p = ldstr(wa0 + wo + 9216); f.u0[2] = p.x; f.u0[3] = p.y; f.x1 = ldsr<u32x4>(xv + 4096); }
    if (N == 3) f.g1 = ldstr2(wa1 + wo, wa1 + wo + 1024);
    if (N == 4) f.u1 = ldstr2(wa1 + wo + 8192, wa1 + wo + 9216);
}
template <int J, int S, bool RD, bool FIRST, class F> DI void g1sub(f32x16 (&ac)[8], const G1Frag& f, G1Frag& n, const u32 (&xa)[4], u32 wa0, u32 wa1, F fill) {
#define G1M(i, a, b) do { if (FIRST) MF32Z(ac[i], a, b); else MF32(ac[i], a, b); } while (0)
    G1M(0, f.g0, f.x0); if (RD) g1rd<J, S, 0>(n, xa, wa0, wa1);
    G1M(1, f.u0, f.x0); if (RD) g1rd<J, S, 1>(n, xa, wa0, wa1);
    G1M(2, f.g0, f.x1); if (RD) g1rd<J, S, 2>(n, xa, wa0, wa1);
    G1M(3, f.u0, f.x1); if (RD) g1rd<J, S, 3>(n, xa, wa0, wa1);
    G1M(4, f.g1, f.x0); if (RD) g1rd<J, S, 4>(n, xa, wa0, wa1);
    G1M(5, f.u1, f.x0); fill(0);
    G1M(6, f.g1, f.x1); fill(1);
    G1M(7, f.u1, f.x1); fill(2);
#undef G1M
}
constexpr u32 rslot(int u) { return u == 0 ? XS0 + 16384 : u == 1 ? WS0 + 32768 : u == 2 ? WS0 + 49152 : u == 3 ? XS0 + 32768 : u == 4 ? WS0 : u == 5 ? WS0 + 16384 : XS0; }
struct WdF { u32x4 a, b; };
template <int I> DI WdF wdread(u32 ta0, u32 ta1) {
    constexpr u32 sl = rslot(I >> 1), hi = sl >= 49152 ? 49152 : 0, o = sl - hi + (I & 1) * 2048;
    u32 b0 = ta0, b1 = ta1;
    if (hi) { b0 += hi; b1 += hi; asm("" : "+v"(b0), "+v"(b1)); }
    return WdF{ldstr2(b0 + o, b0 + o + 1024), ldstr2(b1 + o, b1 + o + 1024)};
}
constexpr int HW = 7;

__global__ __launch_bounds__(512, 2) void fused_kernel(Params P) {
    const int tid = threadIdx.x, lane = tid & 63;
    const int wave = __builtin_amdgcn_readfirstlane(tid >> 6);
    const int xcd = blockIdx.x & 7, r_ = blockIdx.x >> 3;
    const rsrc_t rWS = __builtin_amdgcn_make_buffer_rsrc(P.ws, 0, WS_END, 0x00020000);
    const rsrc_t rX = __builtin_amdgcn_make_buffer_rsrc((void*)P.x, 0, 1835008, 0x00020000);
    if (r_ >= 28) { helper_wg(P, (r_ - 28) * 8 + xcd, tid, lane, wave, rWS, rX); return; }

    const int mi = (xcd >> 1) * 4 + r_ / 7, nj = r_ % 7, fj = (xcd & 1) * 7 + nj, mt = 2 * mi + (xcd & 1);
    const int cg = xcd * 28 + r_;
    const int w4 = wave & 3;
    u32x4 xres;
    u32 tsv = 0;
    STAMP(0); CSTAMP(14);
#define STREAM_SETUP() \
    const int ks0 = ROT1;                           \
    const rsrc_t rW = __builtin_amdgcn_make_buffer_rsrc((void*)((w4 >> 1) ? P.Wu : P.Wg), 0, 1605632, 0x00020000); \
    u32 vx[4], vw[4];                               \
    _Pragma("unroll") for (int i = 0; i < 4; ++i) { \
        const int rx = 32 * w4 + 8 * i + (lane >> 3); \
        vx[i] = rx * 896 + (((lane & 7) ^ ((rx >> 1) & 7)) << 4) - 1024 * i; \
        const int rw = 16 * (w4 & 1) + 4 * i + (lane >> 4); \
        vw[i] = rw * 3584 + (((lane & 15) ^ ((rw & 3) << 2)) << 4) - 1024 * i; \
    } \
    auto unitX = [&](int s) { int k = ks0 + s; k -= k >= 7 ? 7 : 0; return Unit{(u32)(XS0 + (s % 3) * 16384 + w4 * 4096), (u32)(mi * 114688 + k * 128)}; }; \
    auto unitW = [&](int j) { int k = ks0 + (j >> 1); k -= k >= 7 ? 7 : 0; return Unit{(u32)(WS0 + (j & 3) * 16384 + w4 * 4096), (u32)((k * 64 + (j & 1) * 32) * 3584 + fj * 256)}; }; \
    auto issueX = [&](int s) { const Unit u = unitX(s); dma4(u.m0, vx[0], vx[1], vx[2], vx[3], rX, u.so); }; \
    auto issueW = [&](int j) { const Unit u = unitW(j); dma4(u.m0, vw[0], vw[1], vw[2], vw[3], rW, u.so); }

    if (wave < 4) {
        u32x2 tag;
        u32x4 ra, rb;
        ld8c(tag, 0, rWS, WS_EPOCH);
        ln_load(ra, rb, blockIdx.x, w4, lane, rX);
        STREAM_SETUP();
        issueX(0); issueW(1);
        asm volatile("s_waitcnt vmcnt(8)" : "+v"(tag), "+v"(ra), "+v"(rb) :: "memory");
        tag = nexttag(tag);
        ln_compute(ra, rb, blockIdx.x, w4, lane, tag.x, rWS);
        const int lh = w4 >> 1, fh = w4 & 1;
        const int r = lane & 31, h = lane >> 5, blk = (lane >> 4) & 1, qq = (lane & 15) >> 2, p = lane & 3;
        u32 xa[4];
#pragma unroll
        for (int s = 0; s < 4; ++s) xa[s] = (64 * lh + r) * 128 + (((2 * s + h) ^ ((r >> 1) & 7)) << 4);
        const u32 wrow = WS0 + (8 * h + qq) * 256 + 8 * (p & 1);
        u32 wa0 = wrow + (((8 * fh + 2 * blk + (p >> 1)) ^ (qq << 2)) << 4);
        u32 wa1 = wrow + (((8 * fh + 4 + 2 * blk + (p >> 1)) ^ (qq << 2)) << 4);
        asm volatile("" : "+v"(wa0), "+v"(wa1), "+v"(xa[0]), "+v"(xa[1]), "+v"(xa[2]), "+v"(xa[3]));
        const rsrc_t rD = __builtin_amdgcn_make_buffer_rsrc((void*)P.Wd, 0, 1605632, 0x00020000);
        u32 vd0 = (lane >> 3) * 896 + (((lane & 7) ^ (((lane >> 4) & 1) << 2)) << 4);
        auto unitD = [&](int u) { return Unit{(u32)(rslot(u % 7) + w4 * 4096), (u32)((448 * w4 + 32 * u) * 896 + nj * 128)}; };
        f32x16 ac[8];
        G1Frag fa, fb;
        STAMP(9);
        TS(0); WAITV(5);
        TS(1); BAR(); TS(2);
        STAMP(1);
        g1rd<0, 0, 0>(fa, xa, wa0, wa1); g1rd<0, 0, 1>(fa, xa, wa0, wa1); g1rd<0, 0, 2>(fa, xa, wa0, wa1); g1rd<0, 0, 3>(fa, xa, wa0, wa1); g1rd<0, 0, 4>(fa, xa, wa0, wa1);
        ln_store(blockIdx.x, w4, lane, rWS);
        Unit uw, ux = unitX(2);
#define PIECE(j, n) do { \
            if ((j) == 0) { if ((n) == 0) piece<0>(ux, vx[0], rX); if ((n) == 1) piece<1>(ux, vx[1], rX); } \
            else if ((n) < 4 && (j) <= 10) piece<(n) & 3>(uw, vw[(n) & 3], rW); \
            else if ((j) <= 9) piece<((j) & 1) * 2 + ((n) & 1)>(ux, vx[((j) & 1) * 2 + ((n) & 1)], rX); \
            else { constexpr int e_ = ((j) - 10) * 6 + (n) - 4; \
                   if (e_ >= 0 && e_ < 4 * EARLYD) piece<e_ & 3>(unitD(e_ >> 2), vd0 + (e_ & 3) * 6144, rD); } } while (0)
#define HALF(J) do { \
            g1sub<J, 1, true, J == 0>(ac, fa, fb, xa, wa0, wa1, [&](int n) { if (n == 0) PIECE(J, (J == 0 ? 0 : 3)); if (n == 1) PIECE(J, (J == 0 ? 1 : 4)); if (n == 2 && J != 0) PIECE(J, 5); }); \
            if (J == 0) WAITV(5); \
            if (J >= 3 && J <= 9) WAITV(12); \
            if (J == 10) asm volatile("s_waitcnt vmcnt(%0)" :: "i"(10 + (EARLYD ? 2 : 0)) : "memory"); \
            if (J == 11) asm volatile("s_waitcnt vmcnt(%0)" :: "i"(4 + (4 * EARLYD < 8 ? 4 * EARLYD : 8)) : "memory"); \
            if (J == 12) asm volatile("s_waitcnt vmcnt(%0)" :: "i"(4 * EARLYD < 14 ? 4 * EARLYD : 14) : "memory"); \
            TS(3 * J + 4); BAR(); TS(3 * J + 5); \
            if (J < 10) uw = unitW(J + 4); \
            if ((J & 1) && J < 9) ux = unitX((J + 5) / 2); \
            if (J == 3 && tid == 0) stflag(P.ws + WS_FX + blockIdx.x * 8, tag); \
            g1sub<(J < 13 ? J + 1 : 0), 0, (J < 13), false>(ac, fb, fa, xa, wa0, wa1, [&](int n) { if (n == 0) PIECE(J + 1, 0); if (n == 1) PIECE(J + 1, 1); if (n == 2) PIECE(J + 1, 2); }); \
        } while (0)
        HALF(0); HALF(1); HALF(2); HALF(3); HALF(4); HALF(5); HALF(6);
        HALF(7); HALF(8); HALF(9); HALF(10); HALF(11); HALF(12);
        {
            const u32 z = 0;
            const u32 c0 = (64 * fh + r) * 4, l0 = (64 * lh + r) * 4;
            u32 a0 = ldsr<u32>(M_CSG + c0), a1 = ldsr<u32>(M_CSG + c0 + 128), b0 = ldsr<u32>(M_CSU + c0), b1 = ldsr<u32>(M_CSU + c0 + 128);
            u32 m0 = ldsr<u32>(M_NM + l0), m1 = ldsr<u32>(M_NM + l0 + 128);
            HALF(13);
            G1Frag f;
            f.g0 = u32x4{h ? z : a0, z, z, z}; f.g1 = u32x4{h ? z : a1, z, z, z}; f.u0 = u32x4{h ? z : b0, z, z, z}; f.u1 = u32x4{h ? z : b1, z, z, z};
            f.x0 = u32x4{h ? z : m0, z, z, z}; f.x1 = u32x4{h ? z : m1, z, z, z};
            asm volatile("s_nop 1" : "+v"(f.g0), "+v"(f.g1), "+v"(f.u0), "+v"(f.u1), "+v"(f.x0), "+v"(f.x1));
            g1sub<0, 0, false, false>(ac, f, fa, xa, wa0, wa1, [&](int) {});
            asm volatile("s_nop 11" : "+v"(ac[0]), "+v"(ac[1]), "+v"(ac[2]), "+v"(ac[3]), "+v"(ac[4]), "+v"(ac[5]), "+v"(ac[6]), "+v"(ac[7]));
        }
        STAMP(2);
        {
            const u32 hb = WS_H + ((2 * mi + lh) * 112 + 8 * fj + 4 * fh) * 2048 + lane * 16;
#pragma unroll
            for (int t = 0; t < 4; ++t)
#pragma unroll
                for (int s = 0; s < 2; ++s) {
                    u32x4 o;
#pragma unroll
                    for (int j = 0; j < 4; ++j) {
                        const int i = 8 * s + 2 * j;
                        o[j] = pkbf(fmaxf(ac[2 * t][i], 0.f) * ac[2 * t + 1][i], fmaxf(ac[2 * t][i + 1], 0.f) * ac[2 * t + 1][i + 1]);
                    }
                    st16c(o, hb, rWS, ((2 * (t >> 1) + s) * 2 + (t & 1)) * 1024);
                }
        }
        STAMP(3);
        u32 vd[4];
#pragma unroll
        for (int i = 0; i < 4; ++i) vd[i] = vd0 + i * 6144;
        auto issueD = [&](int u) { const Unit t = unitD(u); dma4(t.m0, vd[0], vd[1], vd[2], vd[3], rD, t.so); };
#pragma unroll
        for (int u = EARLYD; u < 7; ++u) issueD(u);
        asm volatile("s_waitcnt vmcnt(%0)" :: "i"(4 * (7 - EARLYD)) : "memory");
        if (lane == 0) stflag(P.ws + WS_FH + ((2 * mi + lh) * 32 + 2 * fj + fh) * 8, tag);
        STAMP(11);
        const u32 trow = w4 * 4096 + (4 * h + qq) * 128 + 8 * (p & 1);
        const u32 ta0 = trow + (((2 * blk + (p >> 1)) ^ ((qq >> 1) << 2)) << 4), ta1 = trow + (((4 + 2 * blk + (p >> 1)) ^ ((qq >> 1) << 2)) << 4);
        WdF wd[14];
#define DRAIN(u) do { WAITV(24); wd[2 * u] = wdread<2 * u>(ta0, ta1); wd[2 * u + 1] = wdread<2 * u + 1>(ta0, ta1); \
                      asm volatile("s_waitcnt lgkmcnt(0)" : "+v"(wd[2 * u].a), "+v"(wd[2 * u].b), "+v"(wd[2 * u + 1].a), "+v"(wd[2 * u + 1].b) :: "memory"); issueD(7 + u); } while (0)
        DRAIN(0); DRAIN(1); DRAIN(2); DRAIN(3); DRAIN(4); DRAIN(5); DRAIN(6);
        STAMP(4);
        u32x2 fl;
        const u32 fv = (7 * w4 + lane) * 8;
        ld8c(fl, fv, rWS, WS_FH + mt * 256);
        asm volatile("buffer_load_dwordx4 %0, %1, %2, 0 offen" : "=v"(xres) : "v"((u32)(64 * mt + (tid >> 3)) * 896 + nj * 128 + (tid & 7) * 16), "s"(rX) : "memory");
        asm volatile("s_waitcnt vmcnt(0)" : "+v"(fl), "+v"(xres) :: "memory");
        waitflags(fl, tag, fv, 7, lane, rWS, WS_FH + mt * 256);
        STAMP(5);
        u32x4 hf[HW][2];
        const u32 hv = lane * 16, hs = WS_H + (mt * 112 + 28 * w4) * 2048;
#define HLOAD(i) asm volatile("buffer_load_dwordx4 %0, %2, %3, %4 offen offset:%5 sc1\n\tbuffer_load_dwordx4 %1, %2, %3, %4 offen offset:%6 sc1" \
                    : "=&v"(hf[(i) % HW][0]), "=&v"(hf[(i) % HW][1]) : "v"(hv), "s"(rWS), "s"(hs + ((i) >> 1) * 4096), "i"(((i) & 1) * 2048), "i"(((i) & 1) * 2048 + 1024) : "memory")
        HLOAD(0); HLOAD(1); HLOAD(2); HLOAD(3); HLOAD(4); HLOAD(5); HLOAD(6);
        f32x16 acc[4];
        WdF wl[2];
#define G2STEP(i) do { \
            if ((i) >= 13 && (i) < 27) wl[((i) + 1) & 1] = wdread<((i) >= 13 && (i) < 27 ? (i) - 13 : 0)>(ta0, ta1); \
            asm volatile("s_waitcnt vmcnt(%2)" : "+v"(hf[(i) % HW][0]), "+v"(hf[(i) % HW][1]) : "i"(2 * ((i) + HW <= 28 ? HW - 1 : 27 - (i))) : "memory"); \
            const WdF& w_ = (i) < 14 ? wd[(i) < 14 ? (i) : 0] : wl[(i) & 1]; \
            if ((i) == 0) { MF32Z(acc[0], w_.a, hf[0][0]); MF32Z(acc[1], w_.a, hf[0][1]); MF32Z(acc[2], w_.b, hf[0][0]); MF32Z(acc[3], w_.b, hf[0][1]); } \
            else { MF32(acc[0], w_.a, hf[(i) % HW][0]); MF32(acc[1], w_.a, hf[(i) % HW][1]); MF32(acc[2], w_.b, hf[(i) % HW][0]); MF32(acc[3], w_.b, hf[(i) % HW][1]); } \
            if ((i) + HW < 28) HLOAD((i) + HW < 28 ? (i) + HW : 0); \
        } while (0)
        G2STEP(0);
        STAMP(6);
        G2STEP(1); G2STEP(2); G2STEP(3); G2STEP(4); G2STEP(5); G2STEP(6); G2STEP(7); G2STEP(8); G2STEP(9);
        G2STEP(10); G2STEP(11); G2STEP(12); G2STEP(13); G2STEP(14); G2STEP(15); G2STEP(16); G2STEP(17); G2STEP(18);
        G2STEP(19); G2STEP(20); G2STEP(21); G2STEP(22); G2STEP(23); G2STEP(24); G2STEP(25); G2STEP(26); G2STEP(27);
        asm volatile("s_nop 11" : "+v"(acc[0]), "+v"(acc[1]), "+v"(acc[2]), "+v"(acc[3]));
        STAMP(7);
        {
            const u32 ib = w4 * 4096 + (r >> 4) * 16384 + (r & 15) * 256;
#pragma unroll
            for (int t = 0; t < 4; ++t)
#pragma unroll
                for (int g = 0; g < 4; ++g)
                    ldsw<f32x4>(ib + (t & 1) * 32768 + (((8 * (t >> 1) + 2 * g + h) ^ (r & 7)) << 4), f32x4{acc[t][4 * g], acc[t][4 * g + 1], acc[t][4 * g + 2], acc[t][4 * g + 3]});
        }
    } else {
        const int ch = w4 >> 1, kh = w4 & 1, d = 2 * cg + ch;
        u32x2 tag;
        u32 pA, pB, pR, pI, pdt, pds;
        {
            const u32 po = (d * 256 + 128 * kh + 2 * lane) * 2, pd = d * 2;
            asm volatile("global_load_ushort %0, %6, %8\n\tglobal_load_dword %1, %7, %9\n\tglobal_load_dword %2, %7, %10\n\t"
                         "global_load_dword %3, %7, %11\n\tglobal_load_dword %4, %7, %12\n\tglobal_load_ushort %5, %6, %13"
                         : "=&v"(pdt), "=&v"(pA), "=&v"(pB), "=&v"(pR), "=&v"(pI), "=&v"(pds)
                         : "v"(pd), "v"(po), "s"(P.logdt), "s"(P.logA), "s"(P.Aim), "s"(P.Cr), "s"(P.Ci), "s"(P.Dsk) : "memory");
        }
        ld8c(tag, 0, rWS, WS_EPOCH);
        STREAM_SETUP();
        issueW(0); issueX(1);
        if (tid == 256) ldsw<u32x4>(M_CNT, u32x4{0, 0, 0, 0});
        asm volatile("s_waitcnt vmcnt(8)" : "+v"(tag), "+v"(pdt), "+v"(pA), "+v"(pB), "+v"(pR), "+v"(pI), "+v"(pds) :: "memory");
        tag = nexttag(tag);
        const float L2E = 1.44269504088896f, I2P = 0.15915494309189535f;
        const float dt = rbf(exp_acc(bflo(pdt)));
        Mul2 w1, w8, w256; Cx2 cur;
        {
            const float a0 = -rbf(exp_acc(bflo(pA))) * dt * L2E, a1 = -rbf(exp_acc(bfhi(pA))) * dt * L2E;
            const float b0 = bflo(pB) * dt * I2P, b1 = bfhi(pB) * dt * I2P;
            cexp1(a0, b0, 8.f, w8.r0, w8.n0); cexp1(a1, b1, 8.f, w8.r1, w8.n1);
            cexp1(a0, b0, 1.f, w1.r0, w1.i0); cexp1(a1, b1, 1.f, w1.r1, w1.i1);
            cexp1(a0, b0, 256.f, w256.r0, w256.i0); cexp1(a1, b1, 256.f, w256.r1, w256.i1);
            w1.n0 = -w1.i0; w1.n1 = -w1.i1; w8.i0 = -w8.n0; w8.i1 = -w8.n1; w256.n0 = -w256.i0; w256.n1 = -w256.i1;
        }
        const float dt2 = dt + dt;
        Cx2 hc0 = {bflo(pR) * dt2, bflo(pI) * dt2, bfhi(pR) * dt2, bfhi(pI) * dt2};
        asm volatile("" : "+v"(hc0.r0), "+v"(hc0.i0), "+v"(hc0.r1), "+v"(hc0.i1), "+v"(w1.r0), "+v"(w1.i0), "+v"(w1.n0), "+v"(w1.r1), "+v"(w1.i1), "+v"(w1.n1),
                     "+v"(w8.r0), "+v"(w8.i0), "+v"(w8.n0), "+v"(w8.r1), "+v"(w8.i1), "+v"(w8.n1),
                     "+v"(w256.r0), "+v"(w256.i0), "+v"(w256.n0), "+v"(w256.r1), "+v"(w256.i1), "+v"(w256.n1));
        issueW(2); issueW(3);
        u32x2 L[32], H[21];
#define LO(b) do { cur = cmul2(cur, w8); L[b] = packrow(cur); } while (0)
#define HI(dst, src, w, row) do { dst = cmul2(src, w); H[row] = packrow(dst); } while (0)
        L[0] = u32x2{0x00003f80u, 0x00003f80u};
        cur = Cx2{w8.r0, w8.i0, w8.r1, w8.i1}; L[1] = packrow(cur);
        STAMP(1);
        TS(0); WAITV(12); TS(1); BAR(); TS(2);
        LO(2); LO(3); LO(4);
        TS(3); TS(4); BAR(); TS(5);
        LO(5); LO(6); LO(7); LO(8); LO(9); LO(10);
        TS(6); WAITV(4); TS(7); BAR(); TS(8);
        LO(11); LO(12); LO(13); LO(14); LO(15); LO(16);
        TS(9); WAITV(0); TS(10); BAR(); TS(11);
        LO(17); LO(18); LO(19); LO(20); LO(21); LO(22);
        TS(12); TS(13); BAR(); TS(14);
        LO(23); LO(24); LO(25); LO(26); LO(27); LO(28);
        TS(15); TS(16); BAR(); TS(17);
        Cx2 h2, h4, h6;
        LO(29); LO(30); LO(31);
        H[0] = packrow(hc0);
        HI(cur, hc0, w1, 1); HI(h2, cur, w1, 2); HI(cur, h2, w1, 3);
        TS(18); TS(19); BAR(); TS(20);
        HI(h4, cur, w1, 4); HI(cur, h4, w1, 5); HI(h6, cur, w1, 6); HI(cur, h6, w1, 7); HI(hc0, hc0, w256, 8); HI(h2, h2, w256, 9);
        TS(21); TS(22); BAR(); TS(23);
        HI(h4, h4, w256, 10); HI(h6, h6, w256, 11); HI(hc0, hc0, w256, 12); HI(h4, h4, w256, 13); HI(hc0, hc0, w256, 14); HI(h4, h4, w256, 15);
        TS(24); TS(25); BAR(); TS(26);
        u32x4 gr; u32x2 fx;
        const u32 grv = w4 < 2 ? (u32)(128 * fj + 64 * w4 + lane) * 16 + (WS_CS - WS_ST) : (u32)(64 * mi + lane) * 16;
        ld16c(gr, grv, rWS, WS_ST);
        ld8c(fx, (64 * w4 + lane) * 8, rWS, WS_FX);
        HI(hc0, hc0, w256, 16); HI(hc0, hc0, w256, 17); HI(hc0, hc0, w256, 18); HI(hc0, hc0, w256, 19); HI(hc0, hc0, w256, 20);
        TS(27); TS(28); BAR(); TS(29);
        asm volatile("s_waitcnt vmcnt(0)" : "+v"(gr), "+v"(fx) :: "memory");
        waitflags(fx, tag, (64 * w4 + lane) * 8, 64, lane, rWS, WS_FX);
        TS(30); TS(31); BAR(); TS(32);
        u32x4 ua, ubb;
        {
            const u32 v = (u32)(64 * w4 + (lane >> 1)) * 7168 + (2 * cg + (lane & 1)) * 16;
            ld16c(ua, v, rWS, WS_XNT); ld16c(ubb, v, rWS, WS_XNT + 32 * 7168);
        }
        {
#pragma unroll 1
            for (int it = 0; it < SPIN_MAX; ++it) {
                if (__all(gr[1] == tag.x && gr[3] == tag.x)) break;
                __builtin_amdgcn_s_sleep(1);
                ld16c(gr, grv, rWS, WS_ST); WAITV(0); PIN(gr);
            }
            if (w4 < 2) { ldsw<u32>(M_CSG + (64 * w4 + lane) * 4, gr[0]); ldsw<u32>(M_CSU + (64 * w4 + lane) * 4, gr[2]); }
            else if (w4 == 2) {
                const u32 n0 = (gr[0] & 0xffffu) ^ 0x8000u, n1 = (gr[2] & 0xffffu) ^ 0x8000u;
                ldsw<u32x2>(M_NM + lane * 8, u32x2{n0 | (n0 << 16), n1 | (n1 << 16)});
                const float r0 = __builtin_amdgcn_rcpf(bfhi(gr[0])), r1 = __builtin_amdgcn_rcpf(bfhi(gr[2]));
                ldsw<u32>(M_RS2 + lane * 8, __float_as_uint(r0 * r0)); ldsw<u32>(M_RS2 + lane * 8 + 4, __float_as_uint(r1 * r1));
            }
        }
        TS(33); TS(34); BAR(); TS(35);
        const u32 sw = STG + w4 * 8704 + lane * 8;
        const u32 sr = STG + w4 * 8704 + (lane & 15) * 544 + (lane >> 4) * 16;
        u32x4 lo[2][8];
#define PUT(i, v) (*(volatile u32x2 LAS*)(uintptr_t)(sw + (i) * 544) = (v))
        STAMP(9);
        TS(36); TS(37); BAR(); TS(38);
#pragma unroll
        for (int i = 0; i < 10; ++i) PUT(i, L[i]);
        TS(39); TS(40); BAR(); TS(41);
#pragma unroll
        for (int i = 10; i < 16; ++i) PUT(i, L[i]);
#pragma unroll
        for (int k = 0; k < 8; ++k) lo[0][k] = *(const volatile u32x4 LAS*)(uintptr_t)(sr + k * 64);
        TS(42); TS(43); BAR(); TS(44);
        STAMP(2);
#pragma unroll
        for (int i = 0; i < 16; ++i) PUT(i, L[16 + i]);
#pragma unroll
        for (int k = 0; k < 8; ++k) lo[1][k] = *(const volatile u32x4 LAS*)(uintptr_t)(sr + k * 64);
        f32x4 kacc[2][2];
#pragma unroll
        for (int th = 0; th < 2; ++th) {
#pragma unroll
            for (int i = 0; i < (th ? 5 : 16); ++i) PUT(i, H[16 * th + i]);
            u32x4 a[8];
#pragma unroll
            for (int k = 0; k < 8; ++k) a[k] = *(const volatile u32x4 LAS*)(uintptr_t)(sr + k * 64);
            kacc[th][0] = f32x4{0.f, 0.f, 0.f, 0.f}; kacc[th][1] = f32x4{0.f, 0.f, 0.f, 0.f};
#pragma unroll
            for (int k = 0; k < 8; ++k) { kacc[th][0] = mfma16(a[k], lo[0][k], kacc[th][0]); kacc[th][1] = mfma16(a[k], lo[1][k], kacc[th][1]); }
        }
        {
            const u32 e = STG + w4 * 8704 + lane * 16;
            if (kh) { ldsw<f32x4>(e, kacc[0][0]); ldsw<f32x4>(e + 1024, kacc[0][1]); }
            else { ldsw<f32x4>(e, kacc[1][0]); ldsw<f32x4>(e + 1024, kacc[1][1]); }
        }
        STAMP(10);
        lds_arrive(M_CNT, lane); lds_wait(M_CNT, 4);
        const u32 dup = UB + 2048 + ch * 3200;
        {
            const u32 e = STG + (w4 ^ 1) * 8704 + lane * 16;
            const f32x4 t0 = ldsr<f32x4>(e), t1 = ldsr<f32x4>(e + 1024);
            const int b = lane & 15, q = lane >> 4;
            if (kh == 0) {
                const int sh = q < 2 ? 5 : 6 - q, bs = q < 2 ? 16 * q : 512 * q;
                const u32 aA = dup + (b << sh) + bs, aB = aA + (q == 3 ? 256 : 8), st = 16 << sh;
                const f32x4 u0 = t0 + kacc[0][0], u1 = t1 + kacc[0][1];
                ldsw<u32x2>(aA, u32x2{pkbf(u0[0], u0[0]), pkbf(u0[1], u0[1])}); ldsw<u32x2>(aB, u32x2{pkbf(u0[2], u0[2]), pkbf(u0[3], u0[3])});
                ldsw<u32x2>(aA + st, u32x2{pkbf(u1[0], u1[0]), pkbf(u1[1], u1[1])}); ldsw<u32x2>(aB + st, u32x2{pkbf(u1[2], u1[2]), pkbf(u1[3], u1[3])});
            } else {
                const u32 aC = dup + (min(512 + 128 * q, 672) + b) * 4;
                const f32x4 u0 = t0 + kacc[1][0], u1 = t1 + kacc[1][1];
#pragma unroll
                for (int i = 0; i < 4; ++i) { ldsw<u32>(aC + i * 128, pkbf(u0[i], u0[i])); ldsw<u32>(aC + i * 128 + 64, pkbf(u1[i], u1[i])); }
            }
        }
        lds_arrive(M_CNT, lane); lds_wait(M_CNT, 8);
        const u32 kc = KC + ch * 8320, ub = UB + ch * 4608;
        {
            const u32 M = 0x0000ffffu;
            u32x4 o;
            const u32 a3 = dup + (512 + 64 * kh + lane) * 4;
            const u32 D0 = ldsr<u32>(a3), D1 = ldsr<u32>(a3 + 4);
            if (kh == 0) {
                const bool s0 = lane < 32;
                const u32 a = dup + (s0 ? 8 * lane : 128 + 4 * lane) * 4;
                const u32x4 A = ldsr<u32x4>(a), B = ldsr<u32x4>(a + 16);
                o[0] = ((s0 ? B[3] : B[0]) & M) | ((s0 ? B[2] : A[3]) & ~M);
                o[1] = s0 ? (B[1] & M) | (B[0] & ~M) : A[2];
                o[2] = ((s0 ? A[3] : A[2]) & M) | ((s0 ? A[2] : A[1]) & ~M);
                o[3] = s0 ? (A[1] & M) | (A[0] & ~M) : A[0];
                if (lane < 8) ldsw<u32x4>(kc + (lane >> 2) * 4160 + 4096 + (lane & 3) * 16, u32x4{0, 0, 0, 0});
            } else {
                const u32 a = dup + (384 + 2 * lane) * 4;
                const u32x2 A = ldsr<u32x2>(a); const u32 B = ldsr<u32>(a + 8);
                o = u32x4{B, A[1], (A[1] & M) | (A[0] & ~M), A[0]};
            }
            const u32 ao = kc + (127 - (64 * kh + lane)) * 16;
            ldsw<u32x4>(ao + 2048, o);
            ldsw<u32x4>(ao, u32x4{D1, (lane & 1) ? D1 : (D1 & M) | (D0 & ~M), D0, D0});
        }
        lds_arrive(M_CNT, lane);
        asm volatile("s_waitcnt vmcnt(0)" : "+v"(ua), "+v"(ubb) :: "memory");
        lds_wait(M_CNT, 12);
        {
            const u32 a = kc + (64 * kh + lane) * 16;
            const u32x4 E0 = ldsr<u32x4>(a), E1 = ldsr<u32x4>(a + 2048); const u32 F0 = ldsr<u32>(a + 16), F1 = ldsr<u32>(a + 2064);
            const u32 au = UB + (lane & 1) * 4608 + 512 + (64 * w4 + (lane >> 1)) * 16;
            ldsw<u32x4>(au, ua); ldsw<u32x4>(au + 512, ubb);
            if (lane < 16) ldsw<u32x4>(ub + kh * 256 + lane * 16, u32x4{0, 0, 0, 0});
            ldsw<u32x4>(a + 4160, u32x4{__builtin_amdgcn_alignbit(E0[1], E0[0], 16), __builtin_amdgcn_alignbit(E0[2], E0[1], 16),
                                        __builtin_amdgcn_alignbit(E0[3], E0[2], 16), __builtin_amdgcn_alignbit(F0, E0[3], 16)});
            ldsw<u32x4>(a + 2048 + 4160, u32x4{__builtin_amdgcn_alignbit(E1[1], E1[0], 16), __builtin_amdgcn_alignbit(E1[2], E1[1], 16),
                                               __builtin_amdgcn_alignbit(E1[3], E1[2], 16), __builtin_amdgcn_alignbit(F1, E1[3], 16)});
        }
        lds_arrive(M_CNT, lane); lds_wait(M_CNT, 16);
        STAMP(3);
        f32x4 y[8];
        {
            const int r = lane & 15, q = lane >> 4, dl = q >> 1, q1 = q & 1;
            u32 va = kc + (1 - (r & 1)) * 4160 + 2 * (2046 - 16 * dl - (r & 14) + 8 * q1 - 128 * kh) - 192 - 3584;
            u32 vb = ub + 512 + 32 * (r - dl) + 16 * q1 - 256 * kh - 192;
            u32 vq[4];
#pragma unroll
            for (int i = 0; i < 4; ++i) vq[i] = va + 1024 * i;
            asm volatile("" : "+v"(vq[0]), "+v"(vq[1]), "+v"(vq[2]), "+v"(vq[3]), "+v"(vb));
            u32x4 A[2][8], B[2][8];
            auto rd = [&](int k, int n, u32x4 (&a)[8], u32x4 (&b)[8]) {
                if (n >= 2 && n < 10) b[n - 2] = *(const volatile u32x4 LAS*)(uintptr_t)(vb + 64 * (3 - k) + 512 * (n - 2));
                else {
                    const int i = n < 2 ? 0 : (n - 8) >> 1;
                    typedef __attribute__((ext_vector_type(2), aligned(4))) u32 u32x2a;
                    const u32x2 p = *(const volatile u32x2a LAS*)(uintptr_t)(vq[(7 - i) >> 1] + 64 * (3 - k) + 512 * ((7 - i) & 1) + 8 * (n & 1));
                    if (n & 1) { a[i][2] = p.x; a[i][3] = p.y; } else { a[i][0] = p.x; a[i][1] = p.y; }
                }
            };
#pragma unroll
            for (int n = 0; n < 24; ++n) rd(0, n, A[0], B[0]);
#pragma unroll
            for (int k = 0; k < 4; ++k) {
                int n = 0;
#pragma unroll
                for (int i = 0; i < 8; ++i)
#pragma unroll
                    for (int t = i; t < 8; ++t) {
                        if (k == 0 && i == 0) asm volatile("v_mfma_f32_16x16x32_bf16 %0, %1, %2, 0" : "=&v"(y[t]) : "v"(A[0][0]), "v"(B[0][t]));
                        else asm volatile("v_mfma_f32_16x16x32_bf16 %0, %1, %2, %0" : "+v"(y[t]) : "v"(A[k & 1][i]), "v"(B[k & 1][t - i]));
                        if (k < 3 && n < 24) rd(k + 1, n, A[(k + 1) & 1], B[(k + 1) & 1]);
                        ++n;
                    }
            }
            asm volatile("s_nop 7" : "+v"(y[0]), "+v"(y[1]), "+v"(y[2]), "+v"(y[3]), "+v"(y[4]), "+v"(y[5]), "+v"(y[6]), "+v"(y[7]));
        }
        STAMP(4);
        u32x2 uu[4];
        {
            const u32 e = EXC + w4 * 4096 + lane * 16;
            if (kh) {
#pragma unroll
                for (int t = 0; t < 4; ++t) ldsw<f32x4>(e + t * 1024, y[t]);
#pragma unroll
                for (int t = 0; t < 4; ++t) y[t] = y[4 + t];
            } else {
#pragma unroll
                for (int t = 0; t < 4; ++t) ldsw<f32x4>(e + t * 1024, y[4 + t]);
            }
            const u32 a = ub + 512 + (1024 * kh + 16 * (lane & 15) + 4 * (lane >> 4)) * 2;
#pragma unroll
            for (int t = 0; t < 4; ++t) uu[t] = ldsr<u32x2>(a + t * 512);
        }
        lds_arrive(M_CNT + 4, lane);
        {
            const float ds = bflo(pds);
#pragma unroll
            for (int t = 0; t < 4; ++t) {
                y[t][0] = __builtin_fmaf(bflo(uu[t].x), ds, y[t][0]); y[t][1] = __builtin_fmaf(bfhi(uu[t].x), ds, y[t][1]);
                y[t][2] = __builtin_fmaf(bflo(uu[t].y), ds, y[t][2]); y[t][3] = __builtin_fmaf(bfhi(uu[t].y), ds, y[t][3]);
            }
        }
        lds_wait(M_CNT + 4, 4);
        {
            const u32 e = EXC + (w4 ^ 1) * 4096 + lane * 16;
            u32x2 z[4];
#pragma unroll
            for (int t = 0; t < 4; ++t) {
                const f32x4 v = ldsr<f32x4>(e + t * 1024);
                z[t] = u32x2{pkbf(addf(v[0], y[t][0]), addf(v[1], y[t][1])), pkbf(addf(v[2], y[t][2]), addf(v[3], y[t][3]))};
            }
            const int q = lane >> 4;
#pragma unroll
            for (int t = 0; t < 4; t += 2) {
                auto a = __builtin_amdgcn_permlane16_swap(z[t].x, z[t + 1].x, false, false);
                auto b = __builtin_amdgcn_permlane16_swap(z[t].y, z[t + 1].y, false, false);
                st16c(u32x4{a[0], b[0], a[1], b[1]}, (u32)(1024 * kh + 256 * (t + (q & 1)) + 16 * (lane & 15) + 8 * (q >> 1)) * 2, rWS, WS_ZT + d * 4096);
            }
        }
        asm volatile("buffer_load_dwordx4 %0, %1, %2, 0 offen" : "=v"(xres) : "v"((u32)(64 * mt + (tid >> 3)) * 896 + nj * 128 + (tid & 7) * 16), "s"(rX) : "memory");
        asm volatile("s_waitcnt vmcnt(0)" : "+v"(xres) :: "memory");
        if (lane == 0) stflag(P.ws + WS_FZ + (2 * d + kh) * 8, tag);
        STAMP(5);
        {
            const u32 fv = (u32)(2 * (64 * nj + 16 * w4 + (lane & 15)) + (mt >> 4)) * 8;
            u32x2 fz;
            ld8c(fz, fv, rWS, WS_FZ);
            asm volatile("s_waitcnt vmcnt(0)" : "+v"(fz) :: "memory");
            waitflags(fz, tag, fv, 64, lane, rWS, WS_FZ);
            const u32 v = (u32)(64 * nj + 16 * w4 + (lane >> 3)) * 4096 + 128 * mt + (lane & 7) * 16;
            dma1c(ZTL + (2 * w4) * 1040, v, rWS, WS_ZT);
            dma1c(ZTL + (2 * w4 + 1) * 1040, v, rWS, WS_ZT + 8 * 4096);
            WAITV(0);
        }
        STAMP(6);
    }
    BAR();
    {
        const int l = tid >> 3, dc = tid & 7;
        const u32 i0 = (l >> 4) * 16384 + (l & 15) * 256 + (((2 * dc) ^ (l & 7)) << 4), i1 = i0 ^ 16;
        f32x4 a = ldsr<f32x4>(i0), b = ldsr<f32x4>(i1);
#pragma unroll
        for (int w = 1; w < 4; ++w) { a += ldsr<f32x4>(i0 + w * 4096); b += ldsr<f32x4>(i1 + w * 4096); }
        const float rs2 = ldsr<float>(M_RS2 + (64 * (xcd & 1) + l) * 4);
        float o[8];
#pragma unroll
        for (int e = 0; e < 8; ++e) {
            const float z = bflo(ldsr<u16>(ZTL + dc * 1040 + e * 128 + l * 2));
            const float xr = (e & 1) ? bfhi(xres[e >> 1]) : bflo(xres[e >> 1]);
            o[e] = __builtin_fmaf(e < 4 ? a[e & 3] : b[e & 3], rs2, xr + z);
        }
        *(u32x4*)(P.out + (size_t)(64 * mt + l) * 896 + nj * 128 + dc * 16) = u32x4{pkbf(o[0], o[1]), pkbf(o[2], o[3]), pkbf(o[4], o[5]), pkbf(o[6], o[7])};
    }
    STAMP(8); CSTAMP(15); TSDUMP();
}

extern "C" void kernel_launch(void* const* d_in, const int* in_sizes, int n_in, void* d_out, int out_size, void* d_ws, size_t ws_size,
                              hipStream_t stream) {
    Params P;
    P.x = (const char*)d_in[0]; P.logA = (const char*)d_in[1]; P.Aim = (const char*)d_in[2]; P.Cr = (const char*)d_in[3];
    P.Ci = (const char*)d_in[4]; P.logdt = (const char*)d_in[5]; P.Dsk = (const char*)d_in[6];
    P.Wg = (const char*)d_in[8]; P.Wu = (const char*)d_in[9]; P.Wd = (const char*)d_in[10];
    P.out = (char*)d_out; P.ws = (char*)d_ws;
    (void)hipFuncSetAttribute((const void*)fused_kernel, hipFuncAttributeMaxDynamicSharedMemorySize, LDS_BYTES);
    hipLaunchKernelGGL(fused_kernel, dim3(256), dim3(512), LDS_BYTES, stream, P);
}
```

```cpp
#include <hip/hip_runtime.h>
#include <stdint.h>

typedef unsigned u32;
typedef unsigned long long u64;
typedef unsigned short u16;
typedef __attribute__((ext_vector_type(2))) u32 u32x2;
typedef __attribute__((ext_vector_type(4))) u32 u32x4;
typedef __attribute__((ext_vector_type(2))) float f32x2;
typedef __attribute__((ext_vector_type(4))) float f32x4;
typedef __attribute__((ext_vector_type(16))) float f32x16;
typedef __attribute__((ext_vector_type(8))) __bf16 bf16x8;
typedef __attribute__((ext_vector_type(2))) __bf16 bf16x2;
typedef __attribute__((ext_vector_type(4))) short s16x4;
typedef __amdgpu_buffer_rsrc_t rsrc_t;
#define LAS __attribute__((address_space(3)))
#define GAS __attribute__((address_space(1)))
#define DI __device__ __forceinline__

constexpr u32 WS_EPOCH = 0, WS_FX = 256, WS_ST = 18432, WS_CS = 34816;
constexpr u32 WS_XNT = 65536, WS_ZT = WS_XNT + 1835008, WS_H = WS_ZT + 3670016, WS_WDT = WS_H + 7340032, WS_FH = WS_WDT + 917504, WS_FW = WS_FH + 114688, WS_END = WS_FW + 4096;
constexpr u32 XS0 = 0, WS0 = 49152, SSM = 114688;
constexpr u32 UB = WS0 + 49152;
constexpr u32 STG01 = XS0, STG23 = SSM;
constexpr u32 EXC = SSM;
constexpr u32 UT = SSM + 16384;
constexpr u32 KN = SSM + 33024;
constexpr u32 MISC = SSM + 42496;
constexpr u32 M_CNT = MISC;
constexpr u32 ZTL = UT, ZT2 = KN;
constexpr u32 LDS_BYTES = 163840;
constexpr int SPIN_MAX = 1 << 18;
#define ROT1 (xcd % 7)
#define EARLYD 5
#define ZSLEEP 8
#define NPOST 1
#define ZSTEP 16
#define CPF 2
#define LATEW 2
#define PACE 4

struct Params {
    const char* x; const char* logA; const char* Aim; const char* Cr; const char* Ci; const char* logdt; const char* Dsk;
    const char* Wg; const char* Wu; const char* Wd; char* out; char* ws;
};

DI float bflo(u32 v) { return __uint_as_float(v << 16); }
DI float bfhi(u32 v) { return __uint_as_float(v & 0xffff0000u); }
DI u32 pkbf(float a, float b) { bf16x2 v = {(__bf16)a, (__bf16)b}; return __builtin_bit_cast(u32, v); }
DI float rbf(float a) { return bflo(pkbf(a, 0.f)); }
DI bf16x8 asbf(u32x4 v) { return __builtin_bit_cast(bf16x8, v); }
template <class T> DI T ldsr(u32 a) { return *(const T LAS*)(uintptr_t)a; }
template <class T> DI void ldsw(u32 a, T v) { *(T LAS*)(uintptr_t)a = v; }
DI u32x2 ldstr(u32 a) { return __builtin_bit_cast(u32x2, __builtin_amdgcn_ds_read_tr16_b64_v4i16((s16x4 LAS*)(uintptr_t)a)); }
DI u32x4 ldstr2(u32 a, u32 b) { u32x2 p = ldstr(a), q = ldstr(b); return u32x4{p.x, p.y, q.x, q.y}; }
#define WAITV(n) asm volatile("s_waitcnt vmcnt(" #n ")" ::: "memory")
#define WAITV2(n) asm volatile("s_waitcnt vmcnt(%0)" :: "i"(n) : "memory")
#define BAR() asm volatile("s_waitcnt lgkmcnt(0)\n\ts_barrier" ::: "memory")
#define SB() __builtin_amdgcn_sched_barrier(0)

DI void dma4(u32 m0v, u32 v0, u32 v1, u32 v2, u32 v3, rsrc_t r, u32 soff) {
    asm volatile("s_mov_b32 m0, %0\n\ts_nop 0\n\t"
                 "buffer_load_dwordx4 %1, %5, %6 offen lds\n\t"
                 "buffer_load_dwordx4 %2, %5, %6 offen offset:1024 lds\n\t"
                 "buffer_load_dwordx4 %3, %5, %6 offen offset:2048 lds\n\t"
                 "buffer_load_dwordx4 %4, %5, %6 offen offset:3072 lds"
                 :: "s"(m0v), "v"(v0), "v"(v1), "v"(v2), "v"(v3), "s"(r), "s"(soff) : "memory");
}
DI void dma1c(u32 m0v, u32 v, rsrc_t r, u32 soff) {
    asm volatile("s_mov_b32 m0, %0\n\ts_nop 0\n\t"
                 "buffer_load_dwordx4 %1, %2, %3 offen sc1 lds"
                 :: "s"(m0v), "v"(v), "s"(r), "s"(soff) : "memory");
}
DI void ld16c(u32x4& d, u32 v, rsrc_t r, u32 soff) { asm volatile("buffer_load_dwordx4 %0, %1, %2, %3 offen sc1" : "=v"(d) : "v"(v), "s"(r), "s"(soff) : "memory"); }
DI void ld4c(u32& d, u32 v, rsrc_t r, u32 soff) { asm volatile("buffer_load_dword %0, %1, %2, %3 offen sc1" : "=v"(d) : "v"(v), "s"(r), "s"(soff) : "memory"); }
DI void ld8c(u32x2& d, u32 v, rsrc_t r, u32 soff) { asm volatile("buffer_load_dwordx2 %0, %1, %2, %3 offen sc1" : "=v"(d) : "v"(v), "s"(r), "s"(soff) : "memory"); }
#define PIN(x) asm volatile("" : "+v"(x) :: "memory")
DI void st16c(u32x4 d, u32 v, rsrc_t r, u32 off) { __builtin_amdgcn_raw_buffer_store_b128(d, r, v + off, 0, 16); }
DI void stflag(char* p, u32x2 tag) { __hip_atomic_store((GAS u64*)(uintptr_t)p, ((u64)tag.y << 32) | tag.x, __ATOMIC_RELAXED, __HIP_MEMORY_SCOPE_AGENT); }
DI u32x2 nexttag(u32x2 e) { u64 v = (((u64)e.y << 32) | e.x) + 1; return u32x2{(u32)v, (u32)(v >> 32)}; }
DI void waitflags(u32x2 f, u32x2 tag, u32 v, int n, int lane, rsrc_t r, u32 soff) {
#pragma unroll 1
    for (int it = 0; it < SPIN_MAX; ++it) {
        bool ok = (lane >= n) || (f.x == tag.x && f.y == tag.y);
        if (__all(ok)) break;
        __builtin_amdgcn_s_sleep(1);
        ld8c(f, v, r, soff); WAITV(0); PIN(f);
    }
}
DI void lds_arrive(u32 a, int lane) {
    asm volatile("s_waitcnt lgkmcnt(0)" ::: "memory");
    if (lane == 0) __hip_atomic_fetch_add((LAS u32*)(uintptr_t)a, 1u, __ATOMIC_RELAXED, __HIP_MEMORY_SCOPE_WORKGROUP);
}
DI void lds_wait(u32 a, u32 target) {
#pragma unroll 1
    for (int it = 0; it < SPIN_MAX; ++it) {
        u32 c = __hip_atomic_load((LAS u32*)(uintptr_t)a, __ATOMIC_RELAXED, __HIP_MEMORY_SCOPE_WORKGROUP);
        if (__builtin_amdgcn_readfirstlane(c) >= target) break;
    }
    asm volatile("" ::: "memory");
}
DI f32x16 mfma32(u32x4 a, u32x4 b, f32x16 c) { return __builtin_amdgcn_mfma_f32_32x32x16_bf16(asbf(a), asbf(b), c, 0, 0, 0); }
DI f32x4 mfma16(u32x4 a, u32x4 b, f32x4 c) { return __builtin_amdgcn_mfma_f32_16x16x32_bf16(asbf(a), asbf(b), c, 0, 0, 0); }
DI f32x4 sel4(int c, f32x4 a, f32x4 b) { asm("" : "+v"(a), "+v"(b)); return c ? a : b; }
DI float dot2(u32 a, u32 b, float c) { return __builtin_amdgcn_fdot2_f32_bf16(__builtin_bit_cast(bf16x2, a), __builtin_bit_cast(bf16x2, b), c, false); }
template <int C> DI float dppf(float v) { return __int_as_float(__builtin_amdgcn_mov_dpp(__float_as_int(v), C, 0xf, 0xf, true)); }
DI float exp_acc(float x) {
    const float L = 1.44269502162933349609375f, Ll = 1.92596299112661746e-8f;
    float hi = x * L, lo = __builtin_fmaf(x, L, -hi) + x * Ll;
    float e = __builtin_amdgcn_exp2f(hi);
    return __builtin_fmaf(e * lo, 0.693147182464599609375f, e);
}

struct Cx2 { f32x2 a, b; };
DI Cx2 cmul2(const Cx2& v, const Cx2& w) {
    Cx2 t;
    asm("v_pk_mul_f32 %0, %2, %4 op_sel:[0,0] op_sel_hi:[0,1]\n\tv_pk_mul_f32 %1, %3, %5 op_sel:[0,0] op_sel_hi:[0,1]\n\t"
        "v_pk_fma_f32 %0, %2, %4, %0 op_sel:[1,1,0] op_sel_hi:[1,0,1] neg_lo:[0,1,0]\n\tv_pk_fma_f32 %1, %3, %5, %1 op_sel:[1,1,0] op_sel_hi:[1,0,1] neg_lo:[0,1,0]"
        : "=&v"(t.a), "=&v"(t.b) : "v"(v.a), "v"(v.b), "v"(w.a), "v"(w.b));
    return t;
}
struct Unit { u32 m0, so; };
template <int I> DI void piece(const Unit& u, u32 v, rsrc_t r) {
    asm volatile("s_mov_b32 m0, %0\n\ts_nop 0\n\tbuffer_load_dwordx4 %1, %2, %3 offen offset:%4 lds" :: "s"(u.m0), "v"(v), "s"(r), "s"(u.so), "i"(I * 1024) : "memory");
}
DI float addf(float a, float b) { float r; asm("v_add_f32 %0, %1, %2" : "=v"(r) : "v"(a), "v"(b)); return r; }
DI u32x2 packrow(const Cx2& v) { u32x2 o = {pkbf(v.a[0], v.a[1]), pkbf(v.b[0], v.b[1])}; asm volatile("" : "+v"(o)); return o; }
DI f32x2 cexp1(float a2, float br, float p) {
    float e = __builtin_amdgcn_exp2f(a2 * p), ph = __builtin_amdgcn_fractf(br * p);
    return f32x2{e * __builtin_amdgcn_cosf(ph), e * __builtin_amdgcn_sinf(ph)};
}

DI void ln_load(u32x4& a, u32x4& b, int blk, int w4, int lane, rsrc_t rX) {
    const u32 v = lane < 56 ? (u32)(8 * blk + 2 * w4) * 896 + lane * 16 : 0x7fff0000u;
    asm volatile("buffer_load_dwordx4 %0, %2, %3, 0 offen\n\tbuffer_load_dwordx4 %1, %2, %3, 0 offen offset:896" : "=&v"(a), "=&v"(b) : "v"(v), "s"(rX) : "memory");
}
DI float red2(float s0, float s1) {
    auto r = __builtin_amdgcn_permlane32_swap(__float_as_uint(s0), __float_as_uint(s1), false, false);
    float t = __uint_as_float(r[0]) + __uint_as_float(r[1]);
    auto q = __builtin_amdgcn_permlane16_swap(__float_as_uint(t), __float_as_uint(t), false, false);
    t = __uint_as_float(q[0]) + __uint_as_float(q[1]);
    t += dppf<0xB1>(t); t += dppf<0x4E>(t); t += dppf<0x141>(t); t += dppf<0x140>(t);
    return t;
}
DI void ln_compute(u32x4 a, u32x4 b, int blk, int w4, int lane, u32 tagx, rsrc_t rWS) {
    float s0 = 0.f, s1 = 0.f;
#pragma unroll
    for (int j = 0; j < 4; ++j) { s0 = dot2(a[j], 0x3f803f80u, s0); s1 = dot2(b[j], 0x3f803f80u, s1); }
    const float mean = red2(s0, s1) * (1.f / 448.f), mb = rbf(mean);
    auto mm = __builtin_amdgcn_permlane32_swap(__float_as_uint(mb), __float_as_uint(mb), false, false);
    const float m0 = __uint_as_float(mm[0]), m1 = __uint_as_float(mm[1]);
    float q0 = 0.f, q1 = 0.f;
#pragma unroll
    for (int j = 0; j < 4; ++j) {
        float c = bflo(a[j]) - m0, d = bfhi(a[j]) - m0, e = bflo(b[j]) - m1, f = bfhi(b[j]) - m1;
        q0 = __builtin_fmaf(c, c, q0); q0 = __builtin_fmaf(d, d, q0); q1 = __builtin_fmaf(e, e, q1); q1 = __builtin_fmaf(f, f, q1);
        a[j] = pkbf(c, d); b[j] = pkbf(e, f);
    }
    if (lane >= 56) { q0 = 0.f; q1 = 0.f; }
    const float dm = mean - mb;
    const float var = rbf(fmaxf(red2(q0, q1) * (1.f / 448.f) - dm * dm, 0.f));
    const float sd = rbf(__builtin_sqrtf(rbf(var + 1e-5f)));
    auto pp = __builtin_amdgcn_permlane32_swap(pkbf(mb, sd), pkbf(mb, sd), false, false);
    if (lane == 0) st16c(u32x4{pp[0], tagx, pp[1], tagx}, (u32)(4 * blk + w4) * 16, rWS, WS_ST);
    const float r0 = __builtin_amdgcn_rcpf(bfhi(pp[0])), r1 = __builtin_amdgcn_rcpf(bfhi(pp[1]));
    u32x4 o, p;
#pragma unroll
    for (int j = 0; j < 4; ++j) { o[j] = pkbf(bflo(a[j]) * r0, bfhi(a[j]) * r0); p[j] = pkbf(bflo(b[j]) * r1, bfhi(b[j]) * r1); }
    if (lane < 56) { ldsw<u32x4>(UB + 2 * w4 * 912 + lane * 16, o); ldsw<u32x4>(UB + (2 * w4 + 1) * 912 + lane * 16, p); }
}
DI void ln_store(int blk, int w4, int lane, rsrc_t rWS) {
#pragma unroll
    for (int i = 0; i < 2; ++i) {
        const int it = i == 0 ? w4 : (w4 < 3 ? w4 + 4 : 3);
        const u32 a = UB + ((lane & 15) >> 2) * 912 + (4 * it + (lane >> 4)) * 32 + (lane & 3) * 8;
        st16c(ldstr2(a, a + 4 * 912), (u32)blk * 7168 + it * 1024 + lane * 16, rWS, WS_XNT);
    }
}

DI void helper_wg(const Params& P, int hw, int tid, int lane, int wave, rsrc_t rWS, rsrc_t rX) {

    u32x2 tag = {0, 0};
    u32x4 wv[14];
    if (wave < 4) {
        u32x2 ep; u32x4 ra, rb;
        ld8c(ep, 0, rWS, WS_EPOCH);
        ln_load(ra, rb, blockIdx.x, wave, lane, rX);
        asm volatile("s_waitcnt vmcnt(0)" : "+v"(ep), "+v"(ra), "+v"(rb) :: "memory");
        tag = nexttag(ep);
        if (tid == 0) ldsw<u32x4>(M_CNT, u32x4{0, 0, tag.x, tag.y});
        ln_compute(ra, rb, blockIdx.x, wave, lane, tag.x, rWS);
        BAR();
        ln_store(blockIdx.x, wave, lane, rWS);
    }
    const int c = lane & 7, hwc = hw < 28 ? hw : 0;
    const rsrc_t rD = __builtin_amdgcn_make_buffer_rsrc((void*)P.Wd, 0, 1605632, 0x00020000);
    const rsrc_t rG = __builtin_amdgcn_make_buffer_rsrc((void*)P.Wg, 0, 1605632, 0x00020000), rU = __builtin_amdgcn_make_buffer_rsrc((void*)P.Wu, 0, 1605632, 0x00020000);
    const u32 wo = (u32)(8 * (wave >> 1) + (lane >> 3)) * 3584 + hwc * 128 + c * 16;
    const u32 vd0 = (lane >> 3) * 896 + (((lane & 7) ^ (((lane >> 4) & 1) << 2)) << 4);
    {
        const rsrc_t rW = (wave & 1) ? rU : rG;
#pragma unroll
        for (int i = 0; i < 14; ++i) asm volatile("buffer_load_dwordx4 %0, %1, %2, %3 offen" : "=v"(wv[i]) : "v"(wo), "s"(rW), "s"(i * 32 * 3584) : "memory");
        dma4(8192 + wave * 4096, vd0, vd0 + 6144, vd0 + 12288, vd0 + 18432, rD, (448 * (hwc & 3) + 32 * wave) * 896 + (hwc >> 2) * 128);
    }
    if (wave < 4) {
        WAITV(18);
        if (lane == 0 && __hip_atomic_fetch_add((LAS u32*)(uintptr_t)M_CNT, 1u, __ATOMIC_RELAXED, __HIP_MEMORY_SCOPE_WORKGROUP) == 3) stflag(P.ws + WS_FX + blockIdx.x * 8, tag);

    } else {
        BAR();
        tag = ldsr<u32x2>(M_CNT + 8);
    }
    WAITV(0);
#pragma unroll
    for (int i = 0; i < 14; ++i) PIN(wv[i]);
    if (hw < 28) {
        const int h = lane >> 5, blk = (lane >> 4) & 1, qq = (lane & 15) >> 2, p = lane & 3;
        const u32 trow = 8192 + wave * 4096 + (4 * h + qq) * 128 + 8 * (p & 1);
        const u32 ta0 = trow + (((2 * blk + (p >> 1)) ^ ((qq >> 1) << 2)) << 4), ta1 = trow + (((4 + 2 * blk + (p >> 1)) ^ ((qq >> 1) << 2)) << 4);
        const u32 o = (u32)(hw * 16 + 2 * wave) * 2048 + lane * 16;
        st16c(ldstr2(ta0, ta0 + 1024), o, rWS, WS_WDT); st16c(ldstr2(ta1, ta1 + 1024), o, rWS, WS_WDT + 1024);
        st16c(ldstr2(ta0 + 2048, ta0 + 3072), o, rWS, WS_WDT + 2048); st16c(ldstr2(ta1 + 2048, ta1 + 3072), o, rWS, WS_WDT + 3072);
    }
    if (hw < 28) {
        float a[8];
        u32 c10 = 0x00003f80u;
        asm("" : "+s"(c10));
#pragma unroll
        for (int j = 0; j < 8; ++j) a[j] = 0.f;
#pragma unroll
        for (int i = 0; i < 14; ++i)
#pragma unroll
            for (int j = 0; j < 4; ++j) { a[2 * j] = dot2(wv[i][j], c10, a[2 * j]); a[2 * j + 1] = dot2(wv[i][j], 0x3f800000u, a[2 * j + 1]); }
        float s4[4], t2[2];
#pragma unroll
        for (int j = 0; j < 4; ++j) { auto r = __builtin_amdgcn_permlane32_swap(__float_as_uint(a[j]), __float_as_uint(a[j + 4]), false, false); s4[j] = __uint_as_float(r[0]) + __uint_as_float(r[1]); }
#pragma unroll
        for (int j = 0; j < 2; ++j) { auto r = __builtin_amdgcn_permlane16_swap(__float_as_uint(s4[j]), __float_as_uint(s4[j + 2]), false, false); t2[j] = __uint_as_float(r[0]) + __uint_as_float(r[1]); }
        ldsw<u32x2>((wave >> 1) * 1024 + ((lane >> 3) & 1) * 512 + (wave & 1) * 256 + (8 * c + 2 * ((lane >> 4) & 1) + 4 * (lane >> 5)) * 4, u32x2{__float_as_uint(t2[0]), __float_as_uint(t2[1])});
    }
    BAR();

    if (wave == 0) {
        if (hw < 28) {
            float g = 0.f, u = 0.f;
#pragma unroll
            for (int w = 0; w < 8; ++w) { g += ldsr<float>(w * 512 + lane * 4); u += ldsr<float>(w * 512 + 256 + lane * 4); }
            float gh = rbf(g), uh = rbf(u);
            st16c(u32x4{pkbf(gh, g - gh), tag.x, pkbf(uh, u - uh), tag.x}, (u32)(64 * hw + lane) * 16, rWS, WS_CS);
        } else if (hw == 31) {
            u32x2 f;
#pragma unroll 1
            for (int i = 0; i < 4; ++i) { ld8c(f, lane * 8 + i * 512, rWS, WS_FX); WAITV(0); PIN(f); waitflags(f, tag, lane * 8 + i * 512, 64, lane, rWS, WS_FX); }
            if (lane == 0) stflag(P.ws + WS_EPOCH, tag);
        }
    }

    if (hw < 28) {
        WAITV(0);
        BAR();
        if (tid == 0) stflag(P.ws + WS_FW + hw * 128, tag);
    }

    asm volatile(".Lcode_tail:");
}

#define MF32(acc, a, b) asm volatile("v_mfma_f32_32x32x16_bf16 %0, %1, %2, %0" : "+v"(acc) : "v"(a), "v"(b) : "memory")
#define MF32Z(acc, a, b) asm volatile("v_mfma_f32_32x32x16_bf16 %0, %1, %2, 0" : "=&v"(acc) : "v"(a), "v"(b) : "memory")
struct G1Frag { u32x4 g0, g1, u0, u1, x0, x1; };
template <int J, int S, int N> DI void g1rd(G1Frag& f, const u32 (&xa)[4], u32 wa0, u32 wa1) {
    constexpr u32 wo = (J & 3) * 16384 + S * 4096, xo = XS0 + ((J >> 1) % 3) * 16384;
    const u32 xv = xa[(J & 1) * 2 + S] + xo;
    if (N == 0) f.g0 = ldstr2(wa0 + wo, wa0 + wo + 1024);
    if (N == 1) { f.x0 = ldsr<u32x4>(xv); const u32x2 p = ldstr(wa0 + wo + 8192); f.u0[0] = p.x; f.u0[1] = p.y; }
    if (N == 2) { const u32x2 p = ldstr(wa0 + wo + 9216); f.u0[2] = p.x; f.u0[3] = p.y; f.x1 = ldsr<u32x4>(xv + 4096); }
    if (N == 3) f.g1 = ldstr2(wa1 + wo, wa1 + wo + 1024);
    if (N == 4) f.u1 = ldstr2(wa1 + wo + 8192, wa1 + wo + 9216);
}
template <int J, int S, bool RD, bool FIRST, class F> DI void g1sub(f32x16 (&ac)[8], const G1Frag& f, G1Frag& n, const u32 (&xa)[4], u32 wa0, u32 wa1, F fill) {
#define G1M(i, a, b) do { if (FIRST) MF32Z(ac[i], a, b); else MF32(ac[i], a, b); } while (0)
    G1M(0, f.g0, f.x0); if (RD) g1rd<J, S, 0>(n, xa, wa0, wa1); else fill(3);
    G1M(1, f.u0, f.x0); if (RD) g1rd<J, S, 1>(n, xa, wa0, wa1); else fill(4);
    G1M(2, f.g0, f.x1); if (RD) g1rd<J, S, 2>(n, xa, wa0, wa1); else fill(5);
    G1M(3, f.u0, f.x1); if (RD) g1rd<J, S, 3>(n, xa, wa0, wa1); else fill(6);
    G1M(4, f.g1, f.x0); if (RD) g1rd<J, S, 4>(n, xa, wa0, wa1); else fill(7);
    G1M(5, f.u1, f.x0); fill(0);
    G1M(6, f.g1, f.x1); fill(1);
    G1M(7, f.u1, f.x1); fill(2);
#undef G1M
}
constexpr u32 rslot(int u) { return u == 0 ? XS0 + 16384 : u == 1 ? WS0 + 32768 : u == 2 ? WS0 + 49152 : u == 3 ? XS0 + 32768 : u == 4 ? WS0 : WS0 + 16384; }
struct WdF { u32x4 a, b; };
template <int I> DI WdF wdread(u32 ta0, u32 ta1) {
    constexpr u32 sl = rslot(I >> 1), hi = sl >= 49152 ? 49152 : 0, o = sl - hi + (I & 1) * 2048;
    u32 b0 = ta0, b1 = ta1;
    if (hi) { b0 += hi; b1 += hi; asm("" : "+v"(b0), "+v"(b1)); }
    return WdF{ldstr2(b0 + o, b0 + o + 1024), ldstr2(b1 + o, b1 + o + 1024)};
}
#define HWIN 7
constexpr int HW = HWIN;

constexpr int lateyounger(int i) { int n = 0; for (int k = 0; k < LATEW; ++k) if (2 * k < i && 2 * k >= i - HW) ++n; return n; }
static_assert(16 - 2 * LATEW - HW > 0, "wait counts");
__global__ __launch_bounds__(512, 2) void fused_kernel(Params P) {
    const int tid = threadIdx.x, lane = tid & 63;
    const int wave = __builtin_amdgcn_readfirstlane(tid >> 6);
    const int xcd = blockIdx.x & 7, r_ = blockIdx.x >> 3;
    if (CPF) {
        u64 pc; u32 back, len;
        asm volatile("s_getpc_b64 %0\n.Lpcref%=:\n\ts_mov_b32 %1, .Lpcref%=-_Z12fused_kernel6Params\n\ts_mov_b32 %2, .Lcode_tail-_Z12fused_kernel6Params" : "=s"(pc), "=s"(back), "=s"(len));
        asm volatile("s_mov_b32 m0, %0\n\ts_nop 0\n\tglobal_load_lds_dword %1, %2" :: "s"(EXC + wave * 256), "v"(r_ < CPF ? min((u32)(wave * 32 + (lane & 31)) * 128, len - 4) : 0u), "s"(pc - back) : "memory");
    }
    const rsrc_t rWS = __builtin_amdgcn_make_buffer_rsrc(P.ws, 0, WS_END, 0x00020000);
    const rsrc_t rX = __builtin_amdgcn_make_buffer_rsrc((void*)P.x, 0, 1835008, 0x00020000);
    if (r_ >= 28) { helper_wg(P, (r_ - 28) * 8 + xcd, tid, lane, wave, rWS, rX); return; }

    const int mi = 2 * xcd + r_ / 14, fj = r_ % 14, nj = fj % 7, mt = 2 * mi + fj / 7;
    const int cg = xcd * 28 + r_;
    const int w4 = wave & 3;
    u32x4 xres;
    u32 sres;

#define STREAM_SETUP() \
    const int ks0 = ROT1;                           \
    const rsrc_t rW = __builtin_amdgcn_make_buffer_rsrc((void*)((w4 >> 1) ? P.Wu : P.Wg), 0, 1605632, 0x00020000); \
    u32 vx[4], vw[4];                               \
    _Pragma("unroll") for (int i = 0; i < 4; ++i) { \
        const int rx = 32 * w4 + 8 * i + (lane >> 3); \
        vx[i] = rx * 896 + (((lane & 7) ^ ((rx >> 1) & 7)) << 4) - 1024 * i; \
        const int rw = 16 * (w4 & 1) + 4 * i + (lane >> 4); \
        vw[i] = rw * 3584 + (((lane & 15) ^ ((rw & 3) << 2)) << 4) - 1024 * i; \
    } \
    auto unitX = [&](int s) { int k = ks0 + s; k -= k >= 7 ? 7 : 0; return Unit{(u32)(XS0 + (s % 3) * 16384 + w4 * 4096), (u32)(mi * 114688 + k * 128)}; }; \
    auto unitW = [&](int j) { int k = ks0 + (j >> 1); k -= k >= 7 ? 7 : 0; return Unit{(u32)(WS0 + (j & 3) * 16384 + w4 * 4096), (u32)((k * 64 + (j & 1) * 32) * 3584 + fj * 256)}; }; \
    auto issueX = [&](int s) { const Unit u = unitX(s); dma4(u.m0, vx[0], vx[1], vx[2], vx[3], rX, u.so); }; \
    auto issueW = [&](int j) { const Unit u = unitW(j); dma4(u.m0, vw[0], vw[1], vw[2], vw[3], rW, u.so); }

    if (wave < 4) {
        u32x2 tag;
        u32x4 ra, rb;
        ld8c(tag, 0, rWS, WS_EPOCH);
        ln_load(ra, rb, blockIdx.x, w4, lane, rX);
        STREAM_SETUP();
        issueX(0);
        asm volatile("s_waitcnt vmcnt(4)" : "+v"(tag), "+v"(ra), "+v"(rb) :: "memory");
        tag = nexttag(tag);
        ln_compute(ra, rb, blockIdx.x, w4, lane, tag.x, rWS);
        issueW(1);
        const int lh = w4 >> 1, fh = w4 & 1;
        const int r = lane & 31, h = lane >> 5, blk = (lane >> 4) & 1, qq = (lane & 15) >> 2, p = lane & 3;
        u32 xa[4];
#pragma unroll
        for (int s = 0; s < 4; ++s) xa[s] = (64 * lh + r) * 128 + (((2 * s + h) ^ ((r >> 1) & 7)) << 4);
        const u32 wrow = WS0 + (8 * h + qq) * 256 + 8 * (p & 1);
        u32 wa0 = wrow + (((8 * fh + 2 * blk + (p >> 1)) ^ (qq << 2)) << 4);
        u32 wa1 = wrow + (((8 * fh + 4 + 2 * blk + (p >> 1)) ^ (qq << 2)) << 4);
        asm volatile("" : "+v"(wa0), "+v"(wa1), "+v"(xa[0]), "+v"(xa[1]), "+v"(xa[2]), "+v"(xa[3]));
        const rsrc_t rD = __builtin_amdgcn_make_buffer_rsrc((void*)P.Wd, 0, 1605632, 0x00020000);
        u32 vd[4];
#pragma unroll
        for (int i = 0; i < 4; ++i) vd[i] = (lane >> 3) * 896 + (((lane & 7) ^ (((lane >> 4) & 1) << 2)) << 4) + i * 6144;
        asm volatile("" : "+v"(vd[0]), "+v"(vd[1]), "+v"(vd[2]), "+v"(vd[3]));
        auto unitD = [&](int u) { return Unit{(u32)(rslot(u) + w4 * 4096), (u32)((448 * w4 + 256 + 32 * u) * 896 + nj * 128)}; };
        f32x16 ac[8];
        G1Frag fa, fb;

        WAITV(5);
        BAR();

        g1rd<0, 0, 0>(fa, xa, wa0, wa1); g1rd<0, 0, 1>(fa, xa, wa0, wa1); g1rd<0, 0, 2>(fa, xa, wa0, wa1); g1rd<0, 0, 3>(fa, xa, wa0, wa1); g1rd<0, 0, 4>(fa, xa, wa0, wa1);
        ln_store(blockIdx.x, w4, lane, rWS);
        Unit uw, ux = unitX(2);
#define PIECE(j, n) do { \
            if ((j) == 0) { if ((n) == 0) piece<0>(ux, vx[0], rX); if ((n) == 1) piece<1>(ux, vx[1], rX); } \
            else if ((n) < 4 && (j) <= 10) piece<(n) & 3>(uw, vw[(n) & 3], rW); \
            else if ((j) <= 9) piece<((j) & 1) * 2 + ((n) & 1)>(ux, vx[((j) & 1) * 2 + ((n) & 1)], rX); \
            else { constexpr int e_ = ((j) - 10) * 6 + (n) - 4; \
                   if (e_ >= 0 && e_ < 4 * EARLYD) piece<e_ & 3>(unitD(e_ >> 2), vd[e_ & 3], rD); } } while (0)
#define DP(u, e) piece<e>(unitD(u), vd[e], rD)
#define HALF(J) do { \
            g1sub<J, 1, true, J == 0>(ac, fa, fb, xa, wa0, wa1, [&](int n) { if (n == 0) PIECE(J, (J == 0 ? 0 : 3)); if (n == 1) PIECE(J, (J == 0 ? 1 : 4)); if (n == 2 && J != 0) PIECE(J, 5); }); \
            if (J == 0) WAITV(4); \
            if (J >= 3 && J <= 9) WAITV(12); \
            if (J == 10) asm volatile("s_waitcnt vmcnt(%0)" :: "i"(10 + (EARLYD ? 2 : 0)) : "memory"); \
            if (J == 11) asm volatile("s_waitcnt vmcnt(%0)" :: "i"(4 + (4 * EARLYD < 8 ? 4 * EARLYD : 8)) : "memory"); \
            if (J == 12) WAITV(18); \
            if (J == 13) asm volatile("s_waitcnt vmcnt(12)" : "+v"(cs0), "+v"(cs1), "+v"(st0), "+v"(st1) :: "memory"); \
        BAR(); \
            if (J < 10) uw = unitW(J + 4); \
            if ((J & 1) && J < 9) ux = unitX((J + 5) / 2); \
            if (J == 11) { ld16c(cs0, csv, rWS, WS_CS); ld16c(cs1, csv, rWS, WS_CS + 512); ld8c(st0, stv, rWS, WS_ST); ld8c(st1, stv, rWS, WS_ST + 256); } \
            if (J == 13) corrchk(); \
            if (J == 3 && tid == 0) stflag(P.ws + WS_FX + blockIdx.x * 8, tag); \
            g1sub<(J < 13 ? J + 1 : 0), 0, (J < 13), false>(ac, fb, fa, xa, wa0, wa1, [&](int n) { if (J < 13) { if (n == 0) PIECE(J + 1, 0); if (n == 1) PIECE(J + 1, 1); if (n == 2) PIECE(J + 1, 2); } \
                else { corrop(n); if (n == 6) DP(5, 0); if (n == 7) DP(5, 1); if (n == 0) DP(5, 2); if (n == 1) DP(5, 3); } }); \
        } while (0)
        WdF wd[16];
        const u32 hv = lane * 16, wds = WS_WDT + (nj * 4 + w4) * 32768;
#define WLOAD(u) asm volatile("buffer_load_dwordx4 %0, %4, %5, %6 offen sc1\n\tbuffer_load_dwordx4 %1, %4, %5, %6 offen offset:1024 sc1\n\t" \
                              "buffer_load_dwordx4 %2, %4, %5, %6 offen offset:2048 sc1\n\tbuffer_load_dwordx4 %3, %4, %5, %6 offen offset:3072 sc1" \
                    : "=&v"(wd[2 * (u)].a), "=&v"(wd[2 * (u)].b), "=&v"(wd[2 * (u) + 1].a), "=&v"(wd[2 * (u) + 1].b) : "v"(hv), "s"(rWS), "s"(wds + (u) * 4096) : "memory")
        u32x4 cs0, cs1; u32x2 st0, st1;
        const u32 csv = (u32)(128 * fj + 64 * fh + r) * 16, stv = (u32)(128 * mi + 64 * lh + r) * 8;
        auto corrchk = [&]() {
#pragma unroll 1
            for (int it = 0; it < SPIN_MAX; ++it) {
                if (__all(((cs0[1] ^ tag.x) | (cs0[3] ^ tag.x) | (cs1[1] ^ tag.x) | (cs1[3] ^ tag.x) | (st0.y ^ tag.x) | (st1.y ^ tag.x)) == 0)) break;
                __builtin_amdgcn_s_sleep(1);
                ld16c(cs0, csv, rWS, WS_CS); ld16c(cs1, csv, rWS, WS_CS + 512); ld8c(st0, stv, rWS, WS_ST); ld8c(st1, stv, rWS, WS_ST + 256);
                asm volatile("s_waitcnt vmcnt(0)" : "+v"(cs0), "+v"(cs1), "+v"(st0), "+v"(st1) :: "memory");
            }
        };
        auto corrop = [&](int n) {
            if (n == 3) { fa.g0[0] = cs0[0]; fa.u0[0] = cs0[2]; fa.g1[0] = cs1[0]; fa.u1[0] = cs1[2]; asm volatile("" : "+v"(fa.g0), "+v"(fa.u0), "+v"(fa.g1), "+v"(fa.u1)); }
            if (n == 4) { fa.x0 = u32x4{h ? 0u : (__builtin_amdgcn_perm(st0.x, st0.x, 0x01000100u) ^ 0x80008000u), 0, 0, 0}; asm volatile("" : "+v"(fa.x0)); }
            if (n == 5) { fa.x1 = u32x4{h ? 0u : (__builtin_amdgcn_perm(st1.x, st1.x, 0x01000100u) ^ 0x80008000u), 0, 0, 0}; asm volatile("" : "+v"(fa.x1)); }
        };
        HALF(0); HALF(1); HALF(2); HALF(3); HALF(4); HALF(5); HALF(6);
        HALF(7); HALF(8); HALF(9); HALF(10); HALF(11); HALF(12);
        HALF(13);
        {
            g1sub<0, 0, false, false>(ac, fa, fb, xa, wa0, wa1, [&](int) {});
            asm volatile("s_nop 11" : "+v"(ac[0]), "+v"(ac[1]), "+v"(ac[2]), "+v"(ac[3]), "+v"(ac[4]), "+v"(ac[5]), "+v"(ac[6]), "+v"(ac[7]));
        }

        const u32 hb = WS_H + ((2 * mi + lh) * 112 + 8 * fj + 4 * fh) * 2048 + lane * 16;
        const u32 trow = w4 * 4096 + (4 * h + qq) * 128 + 8 * (p & 1);
        const u32 ta0 = trow + (((2 * blk + (p >> 1)) ^ ((qq >> 1) << 2)) << 4), ta1 = trow + (((4 + 2 * blk + (p >> 1)) ^ ((qq >> 1) << 2)) << 4);
#define EPI(t) do { _Pragma("unroll") for (int s = 0; s < 2; ++s) { u32x4 o; \
                _Pragma("unroll") for (int j = 0; j < 4; ++j) { const int i = 8 * s + 2 * j; \
                    o[j] = pkbf(fmaxf(ac[2 * t][i], 0.f) * ac[2 * t + 1][i], fmaxf(ac[2 * t][i + 1], 0.f) * ac[2 * t + 1][i + 1]); } \
                st16c(o, hb, rWS, ((2 * (t >> 1) + s) * 2 + (t & 1)) * 1024); } SB(); } while (0)
        EPI(0); EPI(1); EPI(2); EPI(3);

        if (NPOST > 0) WLOAD(0);
        if (NPOST > 1) WLOAD(1);
        if (NPOST > 2) WLOAD(2);
        asm volatile("s_waitcnt vmcnt(%0)" :: "i"(4 * NPOST) : "memory");
        if (lane == 0) stflag(P.ws + WS_FH + ((2 * mi + lh) * 28 + 2 * fj + fh) * 128, tag);

        u32x2 fl;
        const u32 fv = (7 * w4 + min(lane, 6)) * 128;
        asm volatile("buffer_load_dwordx4 %0, %1, %2, 0 offen" : "=v"(xres) : "v"((u32)(64 * mt + (tid >> 3)) * 896 + nj * 128 + (tid & 7) * 16), "s"(rX) : "memory");
        if (NPOST < 1) WLOAD(0);
        if (NPOST < 2) WLOAD(1);
        if (NPOST < 3) { WAITV2(PACE); WLOAD(2); }
        WAITV2(PACE); WLOAD(3); WAITV2(PACE); WLOAD(4);
        if (LATEW < 3) { WAITV2(PACE); WLOAD(5); }
        if (LATEW < 2) { WAITV2(PACE); WLOAD(6); }
        if (LATEW < 1) { WAITV2(PACE); WLOAD(7); }
        ld8c(fl, fv, rWS, WS_FH + mt * 3584);

        asm volatile("s_waitcnt vmcnt(0)" : "+v"(fl) :: "memory");
        waitflags(fl, tag, fv, 7, lane, rWS, WS_FH + mt * 3584);

        ld4c(sres, (u32)(64 * mt + (tid >> 3)) * 8, rWS, WS_ST);
        u32x4 hf[HW][2];
        const u32 hs = WS_H + (mt * 112 + 28 * w4) * 2048;
#define HLOAD(i) asm volatile("buffer_load_dwordx4 %0, %2, %3, %4 offen offset:%5 sc1\n\tbuffer_load_dwordx4 %1, %2, %3, %4 offen offset:%6 sc1" \
                    : "=&v"(hf[(i) % HW][0]), "=&v"(hf[(i) % HW][1]) : "v"(hv), "s"(rWS), "s"(hs + ((i) >> 1) * 4096), "i"(((i) & 1) * 2048), "i"(((i) & 1) * 2048 + 1024) : "memory")
        HLOAD(0); HLOAD(1);
        HLOAD(2); HLOAD(3); if (HW > 4) HLOAD(4); if (HW > 5) HLOAD(5); if (HW > 6) HLOAD(6);
        lds_wait(M_CNT, 8);
        f32x16 acc[4];
        WdF wl[2];
#define G2STEP(i) do { \
            if ((i) >= 15 && (i) < 27) wl[((i) + 1) & 1] = wdread<((i) >= 15 && (i) < 27 ? (i) - 15 : 0)>(ta0, ta1); \
            asm volatile("s_waitcnt vmcnt(%2)" : "+v"(hf[(i) % HW][0]), "+v"(hf[(i) % HW][1]) : "i"(2 * ((i) + HW <= 28 ? HW - 1 : 27 - (i)) + 4 * lateyounger(i)) : "memory"); \
            if ((i) >= 16 - 2 * LATEW && (i) < 16) asm volatile("" : "+v"(wd[(i) & 15].a), "+v"(wd[(i) & 15].b)); \
            if ((i) == 0) { PIN(xres); PIN(sres); _Pragma("unroll") for (int k_ = 0; k_ < 16 - 2 * LATEW; ++k_) asm volatile("" : "+v"(wd[k_].a), "+v"(wd[k_].b)); } \
            const WdF& w_ = (i) < 16 ? wd[(i) < 16 ? (i) : 0] : wl[(i) & 1]; \
            if ((i) == 0) { MF32Z(acc[0], w_.a, hf[0][0]); MF32Z(acc[1], w_.a, hf[0][1]); MF32Z(acc[2], w_.b, hf[0][0]); MF32Z(acc[3], w_.b, hf[0][1]); } \
            else { MF32(acc[0], w_.a, hf[(i) % HW][0]); MF32(acc[1], w_.a, hf[(i) % HW][1]); MF32(acc[2], w_.b, hf[(i) % HW][0]); MF32(acc[3], w_.b, hf[(i) % HW][1]); } \
            if ((i) + HW < 28) HLOAD((i) + HW < 28 ? (i) + HW : 0); \
            if ((i) == ZSTEP && lane == 0) __hip_atomic_fetch_add((LAS u32*)(uintptr_t)(M_CNT + 12), 1u, __ATOMIC_RELAXED, __HIP_MEMORY_SCOPE_WORKGROUP); \
            if ((i) < 2 * LATEW && !((i) & 1)) WLOAD(((i) < 2 * LATEW ? 8 - LATEW + (i) / 2 : 0)); \
        } while (0)
        G2STEP(0);

        G2STEP(1); G2STEP(2); G2STEP(3); G2STEP(4); G2STEP(5); G2STEP(6); G2STEP(7); G2STEP(8); G2STEP(9);
        G2STEP(10); G2STEP(11); G2STEP(12); G2STEP(13); G2STEP(14); G2STEP(15); G2STEP(16); G2STEP(17); G2STEP(18);
        G2STEP(19); G2STEP(20); G2STEP(21); G2STEP(22); G2STEP(23); G2STEP(24); G2STEP(25); G2STEP(26); G2STEP(27);
        asm volatile("s_nop 11" : "+v"(acc[0]), "+v"(acc[1]), "+v"(acc[2]), "+v"(acc[3]));

        {
            const u32 ib = w4 * 4096 + r * 128 + 8 * h;
#pragma unroll
            for (int t = 0; t < 4; ++t)
#pragma unroll
                for (int g = 0; g < 4; ++g)
                    *(volatile u32x2 LAS*)(uintptr_t)(ib + (t & 1) * 16384 + (((4 * (t >> 1) + g) ^ (r & 7)) << 4)) = u32x2{pkbf(acc[t][4 * g], acc[t][4 * g + 1]), pkbf(acc[t][4 * g + 2], acc[t][4 * g + 3])};
        }
    } else {
        const int ch = w4 >> 1, kh = w4 & 1, d = 2 * cg + ch;
        u32x2 tag;
        u32 pA, pB, pR, pI, pdt, pds;
        {
            const u32 po = (d * 256 + 128 * kh + 2 * lane) * 2, pd = d * 2;
            asm volatile("global_load_ushort %0, %6, %8\n\tglobal_load_dword %1, %7, %9\n\tglobal_load_dword %2, %7, %10\n\t"
                         "global_load_dword %3, %7, %11\n\tglobal_load_dword %4, %7, %12\n\tglobal_load_ushort %5, %6, %13"
                         : "=&v"(pdt), "=&v"(pA), "=&v"(pB), "=&v"(pR), "=&v"(pI), "=&v"(pds)
                         : "v"(pd), "v"(po), "s"(P.logdt), "s"(P.logA), "s"(P.Aim), "s"(P.Cr), "s"(P.Ci), "s"(P.Dsk) : "memory");
        }
        ld8c(tag, 0, rWS, WS_EPOCH);
        STREAM_SETUP();
        if (tid == 256) ldsw<u32x4>(M_CNT, u32x4{0, 0, 0, 0});
        if (w4 == 2 && lane < 16) ldsw<u32x4>(UT + (lane >> 2) * 4160 + 4096 + (lane & 3) * 16, u32x4{0, 0, 0, 0});
        if (w4 == 3) ldsw<u32x4>(KN + (lane >> 5) * 4608 + (lane & 31) * 16, u32x4{0, 0, 0, 0});
        issueW(0);
        const u32 stg = w4 < 2 ? STG01 + w4 * 8192 : STG23 + (w4 - 2) * 8192;
        u32 sw0 = stg + lane * 16, sw1 = stg + (lane ^ 1) * 16, sw2 = stg + (lane ^ 4) * 16, sw3 = stg + (lane ^ 5) * 16;
        u32 sw4 = stg + (lane ^ 8) * 16, sw5 = stg + (lane ^ 9) * 16, sw6 = stg + (lane ^ 12) * 16, sw7 = stg + (lane ^ 13) * 16;
        u32 sr[4];
        {
            const int r = lane & 15, q = lane >> 4, pp = r >> 1, g = (pp & 1) | ((pp & 6) << 1);
#pragma unroll
            for (int i = 0; i < 4; ++i) sr[i] = stg + pp * 1024 + (((8 * (i >> 1) + 2 * q + (i & 1)) ^ g) << 4) + (r & 1) * 8;
        }
        asm volatile("" : "+v"(sr[0]), "+v"(sr[1]), "+v"(sr[2]), "+v"(sr[3]), "+v"(sw0), "+v"(sw1), "+v"(sw2), "+v"(sw3), "+v"(sw4), "+v"(sw5), "+v"(sw6), "+v"(sw7));
        asm volatile("s_waitcnt vmcnt(4)" : "+v"(tag), "+v"(pdt), "+v"(pA), "+v"(pB), "+v"(pR), "+v"(pI), "+v"(pds) :: "memory");
        tag = nexttag(tag);
        const float L2E = 1.44269504088896f, I2P = 0.15915494309189535f;
        const float dt = rbf(exp_acc(bflo(pdt)));
        Cx2 w1, w8, w256, cur;
        {
            const float a0 = -rbf(exp_acc(bflo(pA))) * dt * L2E, a1 = -rbf(exp_acc(bfhi(pA))) * dt * L2E;
            const float b0 = bflo(pB) * dt * I2P, b1 = bfhi(pB) * dt * I2P;
            w8 = Cx2{cexp1(a0, b0, 8.f), cexp1(a1, b1, 8.f)}; w8.a[1] = -w8.a[1]; w8.b[1] = -w8.b[1];
            w1 = Cx2{cexp1(a0, b0, 1.f), cexp1(a1, b1, 1.f)};
            w256 = Cx2{cexp1(a0, b0, 256.f), cexp1(a1, b1, 256.f)};
        }
        const float dt2 = dt + dt;
        Cx2 hc0 = {f32x2{bflo(pR) * dt2, bflo(pI) * dt2}, f32x2{bfhi(pR) * dt2, bfhi(pI) * dt2}};
#define PUT(p, A, i) (*(volatile u32x4 LAS*)(uintptr_t)(sw##p + (p) * 1024) = u32x4{A[i].x, A[i].y, A[(i) + 1].x, A[(i) + 1].y})
        auto getf = [&](int k) {
            const u32x2 a = *(const volatile u32x2 LAS*)(uintptr_t)(sr[2 * (k & 1)] + (k >> 1) * 256), b = *(const volatile u32x2 LAS*)(uintptr_t)(sr[2 * (k & 1) + 1] + (k >> 1) * 256);
            return u32x4{a.x, a.y, b.x, b.y};
        };
        u32x2 L[32], H[22];
        u32x4 lo[2][8], fa[8], fb[8];
#define LO(b) do { cur = cmul2(cur, w8); L[b] = packrow(cur); } while (0)
#define HI(dst, src, w, row) do { dst = cmul2(src, w); H[row] = packrow(dst); } while (0)
#define GET2(A, k) do { A[k] = getf(k); A[(k) + 1] = getf((k) + 1); } while (0)
        L[0] = u32x2{0x00003f80u, 0x00003f80u};
        cur = w8; L[1] = packrow(cur);

        issueX(1);
#define PUT8(A, i) do { PUT(0, A, i); PUT(1, A, i + 2); PUT(2, A, i + 4); PUT(3, A, i + 6); PUT(4, A, i + 8); PUT(5, A, i + 10); PUT(6, A, i + 12); PUT(7, A, i + 14); } while (0)
#define GET8(A) do { GET2(A, 0); GET2(A, 2); GET2(A, 4); GET2(A, 6); } while (0)
#pragma unroll
        for (int b = 2; b < 32; ++b) LO(b);
        Cx2 h2, h4, h6;
        H[0] = packrow(hc0);
        HI(cur, hc0, w1, 1); HI(h2, cur, w1, 2); HI(cur, h2, w1, 3); HI(h4, cur, w1, 4); HI(cur, h4, w1, 5); HI(h6, cur, w1, 6); HI(cur, h6, w1, 7);
#define HA() do { HI(hc0, hc0, w256, 8); HI(h2, h2, w256, 9); HI(h4, h4, w256, 10); HI(h6, h6, w256, 11); } while (0)
#define HB() do { HI(hc0, hc0, w256, 12); HI(h4, h4, w256, 13); HI(hc0, hc0, w256, 14); HI(h4, h4, w256, 15); } while (0)
#define HC() do { HI(hc0, hc0, w256, 16); HI(hc0, hc0, w256, 17); HI(hc0, hc0, w256, 18); HI(hc0, hc0, w256, 19); HI(hc0, hc0, w256, 20); } while (0)
        H[21] = u32x2{0, 0};

        WAITV(4); BAR();
        issueW(2);
        HA();
        BAR();
        issueW(3);
        HB();
        WAITV(4); BAR();
        HC();
        WAITV(0); BAR();
        BAR();
        BAR();
        BAR();
        BAR();
        BAR();
        BAR();
        BAR();

        BAR();
        u32x2 fx, fw;
        ld8c(fx, (64 * w4 + lane) * 8, rWS, WS_FX);
        BAR();
        asm volatile("s_waitcnt vmcnt(0)" : "+v"(fx) :: "memory");
        waitflags(fx, tag, (64 * w4 + lane) * 8, 64, lane, rWS, WS_FX);
        u32x4 ua, ubb;
        {
            const u32 v = (u32)(64 * w4 + (lane >> 1)) * 7168 + (2 * cg + (lane & 1)) * 16;
            ld16c(ua, v, rWS, WS_XNT); ld16c(ubb, v, rWS, WS_XNT + 32 * 7168);
        }
        ld8c(fw, (4 * nj + w4) * 128, rWS, WS_FW);
        BAR();
        asm volatile("s_waitcnt vmcnt(0)" : "+v"(fw), "+v"(ua), "+v"(ubb) :: "memory");
        waitflags(fw, tag, (4 * nj + w4) * 128, 64, lane, rWS, WS_FW);
        const u32 ut = UT + ch * 8320, kn = KN + ch * 4608;
        u32 vq[4], vb, aA, aB, aC, a3, ae, ac1, ao;
        {
            const int r = lane & 15, q = lane >> 4, dl = q >> 1, q1 = q & 1;
            const u32 va = ut + (1 - (r & 1)) * 4160 + 2 * (2046 - 16 * dl - (r & 14) + 8 * q1 - 128 * kh) - 192 - 3584;
            vb = kn + 512 + 32 * (r - dl) + 16 * q1 - 256 * kh - 192;
#pragma unroll
            for (int i = 0; i < 4; ++i) vq[i] = va + 1024 * i;
            aA = stg + (r << (q < 2 ? 5 : 6 - q)) + (q < 2 ? 16 * q : 512 * q); aB = aA + (q == 3 ? 256 : 8);
            aC = stg + (512 + 32 * min(r, 5) + 4 * q) * 4;
            const u32 pa = stg - kh * 8192;
            a3 = pa + (512 + 64 * kh + lane) * 4;
            ae = kh ? pa + (384 + 2 * lane) * 4 : pa + (lane < 32 ? 8 * lane : 128 + 4 * lane) * 4;
            ac1 = ut + (64 * kh + lane) * 16; ao = kn + 512 + (64 * kh + lane) * 16;
            asm volatile("" : "+v"(vq[0]), "+v"(vq[1]), "+v"(vq[2]), "+v"(vq[3]), "+v"(vb), "+v"(aA), "+v"(aB), "+v"(aC), "+v"(a3), "+v"(ae), "+v"(ac1), "+v"(ao));
        }
        BAR();

        PUT8(L, 0); GET8(lo[0]);
        PUT8(L, 16); GET8(lo[1]);
        PUT8(H, 0); GET8(fa);
        PUT(0, H, 16); PUT(1, H, 18); PUT(2, H, 20); GET8(fb);
        {
            const u32 au = UT + (lane & 1) * 8320 + (255 - (64 * w4 + (lane >> 1))) * 16;
            ldsw<u32x4>(au, u32x4{__builtin_amdgcn_alignbit(ua[3], ua[3], 16), __builtin_amdgcn_alignbit(ua[2], ua[2], 16),
                                  __builtin_amdgcn_alignbit(ua[1], ua[1], 16), __builtin_amdgcn_alignbit(ua[0], ua[0], 16)});
            ldsw<u32x4>(au - 512, u32x4{__builtin_amdgcn_alignbit(ubb[3], ubb[3], 16), __builtin_amdgcn_alignbit(ubb[2], ubb[2], 16),
                                        __builtin_amdgcn_alignbit(ubb[1], ubb[1], 16), __builtin_amdgcn_alignbit(ubb[0], ubb[0], 16)});
        }
        SB();
        f32x4 kacc[2][2];
        {
            const f32x4 zz = {0.f, 0.f, 0.f, 0.f};
            kacc[0][0] = zz; kacc[0][1] = zz; kacc[1][0] = zz; kacc[1][1] = zz;
#pragma unroll
            for (int k = 0; k < 8; ++k) {
                kacc[0][0] = mfma16(fa[k], lo[0][k], kacc[0][0]); kacc[0][1] = mfma16(fa[k], lo[1][k], kacc[0][1]);
                kacc[1][0] = mfma16(lo[0][k], fb[k], kacc[1][0]); kacc[1][1] = mfma16(lo[1][k], fb[k], kacc[1][1]);
                SB();
            }
        }

        {
            const int q = lane >> 4;
            const u32 st = 16 << (q < 2 ? 5 : 6 - q);
            ldsw<u32x2>(aA, u32x2{__float_as_uint(kacc[0][0][0]), __float_as_uint(kacc[0][0][1])}); ldsw<u32x2>(aB, u32x2{__float_as_uint(kacc[0][0][2]), __float_as_uint(kacc[0][0][3])});
            ldsw<u32x2>(aA + st, u32x2{__float_as_uint(kacc[0][1][0]), __float_as_uint(kacc[0][1][1])}); ldsw<u32x2>(aB + st, u32x2{__float_as_uint(kacc[0][1][2]), __float_as_uint(kacc[0][1][3])});
            ldsw<f32x4>(aC, kacc[1][0]); ldsw<f32x4>(aC + 64, kacc[1][1]);
        }

        lds_arrive(M_CNT, lane); lds_wait(M_CNT, 4);
        {
            const u32x4 E0 = ldsr<u32x4>(ac1), E1 = ldsr<u32x4>(ac1 + 2048); const u32 F0 = ldsr<u32>(ac1 + 16), F1 = ldsr<u32>(ac1 + 2064);
            const float d0 = ldsr<float>(a3), d1 = ldsr<float>(a3 + 4), d2 = ldsr<float>(a3 + 8192), d3 = ldsr<float>(a3 + 8196);
            u32x4 o;
            if (kh == 0) {
                const bool s0 = lane < 32;
                const f32x4 A = ldsr<f32x4>(ae) + ldsr<f32x4>(ae + 8192), B = ldsr<f32x4>(ae + 16) + ldsr<f32x4>(ae + 8208);
                o[0] = pkbf(A[0], s0 ? A[1] : A[0]);
                o[1] = pkbf(s0 ? A[2] : A[1], s0 ? A[3] : A[2]);
                o[2] = pkbf(s0 ? B[0] : A[2], s0 ? B[1] : A[2]);
                o[3] = pkbf(s0 ? B[2] : A[3], s0 ? B[3] : B[0]);
            } else {
                const float A0 = ldsr<float>(ae) + ldsr<float>(ae + 8192), A1 = ldsr<float>(ae + 4) + ldsr<float>(ae + 8196), A2 = ldsr<float>(ae + 8) + ldsr<float>(ae + 8200);
                o = u32x4{pkbf(A0, A0), pkbf(A0, A1), pkbf(A1, A1), pkbf(A2, A2)};
            }
            ldsw<u32x4>(ac1 + 4160, u32x4{__builtin_amdgcn_alignbit(E0[1], E0[0], 16), __builtin_amdgcn_alignbit(E0[2], E0[1], 16),
                                          __builtin_amdgcn_alignbit(E0[3], E0[2], 16), __builtin_amdgcn_alignbit(F0, E0[3], 16)});
            ldsw<u32x4>(ac1 + 2048 + 4160, u32x4{__builtin_amdgcn_alignbit(E1[1], E1[0], 16), __builtin_amdgcn_alignbit(E1[2], E1[1], 16),
                                                 __builtin_amdgcn_alignbit(E1[3], E1[2], 16), __builtin_amdgcn_alignbit(F1, E1[3], 16)});
            ldsw<u32x4>(ao, o);
            const float D0 = d0 + d2, D1 = d1 + d3;
            const u32 d00 = pkbf(D0, D0);
            ldsw<u32x4>(ao + 2048, u32x4{d00, d00, pkbf((lane & 1) ? D1 : D0, D1), pkbf(D1, D1)});
        }
        lds_arrive(M_CNT, lane); lds_wait(M_CNT, 8);
        __builtin_amdgcn_s_setprio(2);

        f32x4 y[8];
        {
            u32x4 A[2][8], B[2][8];
            auto rd = [&](int k, int n, u32x4 (&a)[8], u32x4 (&b)[8]) {
                if (n >= 2 && n < 10) b[n - 2] = *(const volatile u32x4 LAS*)(uintptr_t)(vb + 64 * (3 - k) + 512 * (n - 2));
                else {
                    const int i = n < 2 ? 0 : (n - 8) >> 1;
                    typedef __attribute__((ext_vector_type(2), aligned(4))) u32 u32x2a;
                    const u32x2 p = *(const volatile u32x2a LAS*)(uintptr_t)(vq[(7 - i) >> 1] + 64 * (3 - k) + 512 * ((7 - i) & 1) + 8 * (n & 1));
                    if (n & 1) { a[i][2] = p.x; a[i][3] = p.y; } else { a[i][0] = p.x; a[i][1] = p.y; }
                }
            };
#pragma unroll
            for (int n = 0; n < 24; ++n) rd(0, n, A[0], B[0]);
            auto cm = [&](int k, int i, int t) {
                if (k == 0 && i == 0) asm volatile("v_mfma_f32_16x16x32_bf16 %0, %1, %2, 0" : "=&v"(y[t]) : "v"(A[0][0]), "v"(B[0][t]));
                else asm volatile("v_mfma_f32_16x16x32_bf16 %0, %1, %2, %0" : "+v"(y[t]) : "v"(A[k & 1][i]), "v"(B[k & 1][t - i]));
            };
#pragma unroll
            for (int k = 0; k < 4; ++k) {
                int n = 0;
#pragma unroll
                for (int i = 0; i < 5; ++i)
#pragma unroll
                    for (int t = i; t < 8; ++t) {
                        if (k > 0 && i == 0 && t < 5) continue;
                        cm(k, i, t);
                        if (k < 3 && n < 24) rd(k + 1, n, A[(k + 1) & 1], B[(k + 1) & 1]);
                        ++n;
                    }
                if (k < 3) {
                    cm(k, 5, 5); cm(k, 5, 6); cm(k, 5, 7); cm(k + 1, 0, 0); cm(k, 6, 6); cm(k + 1, 0, 1); cm(k, 6, 7); cm(k + 1, 0, 2); cm(k + 1, 0, 3); cm(k, 7, 7); cm(k + 1, 0, 4);
                } else { cm(k, 5, 5); cm(k, 5, 6); cm(k, 5, 7); cm(k, 6, 6); cm(k, 6, 7); cm(k, 7, 7); }
            }
            asm volatile("s_nop 7" : "+v"(y[0]), "+v"(y[1]), "+v"(y[2]), "+v"(y[3]), "+v"(y[4]), "+v"(y[5]), "+v"(y[6]), "+v"(y[7]));
        }

        u32x2 uu[4];
        {
            const u32 e = EXC + w4 * 4096 + lane * 16;
            if (kh) {
#pragma unroll
                for (int t = 0; t < 4; ++t) ldsw<f32x4>(e + t * 1024, y[t]);
#pragma unroll
                for (int t = 0; t < 4; ++t) y[t] = y[4 + t];
            } else {
#pragma unroll
                for (int t = 0; t < 4; ++t) ldsw<f32x4>(e + t * 1024, y[4 + t]);
            }
            const u32 a = ut + (2044 - 768 - (1024 * kh + 16 * (lane & 15) + 4 * (lane >> 4))) * 2;
#pragma unroll
            for (int t = 0; t < 4; ++t) uu[t] = ldsr<u32x2>(a + (3 - t) * 512);
        }
        lds_arrive(M_CNT + 4, lane);
        {
            const float ds = bflo(pds);
#pragma unroll
            for (int t = 0; t < 4; ++t) {
                y[t][0] = __builtin_fmaf(bfhi(uu[t].y), ds, y[t][0]); y[t][1] = __builtin_fmaf(bflo(uu[t].y), ds, y[t][1]);
                y[t][2] = __builtin_fmaf(bfhi(uu[t].x), ds, y[t][2]); y[t][3] = __builtin_fmaf(bflo(uu[t].x), ds, y[t][3]);
            }
        }
        lds_wait(M_CNT + 4, 4);
        {
            const u32 e = EXC + (w4 ^ 1) * 4096 + lane * 16;
            u32x2 z[4];
#pragma unroll
            for (int t = 0; t < 4; ++t) {
                const f32x4 v = ldsr<f32x4>(e + t * 1024);
                z[t] = u32x2{pkbf(addf(v[0], y[t][0]), addf(v[1], y[t][1])), pkbf(addf(v[2], y[t][2]), addf(v[3], y[t][3]))};
            }
#pragma unroll
            for (int t = 0; t < 4; ++t)
                st16c(u32x4{z[t].x, tag.x, z[t].y, tag.x}, (u32)(1024 * kh + 256 * t + 16 * (lane & 15) + 4 * (lane >> 4)) * 4, rWS, WS_ZT + d * 8192);
        }

        __builtin_amdgcn_s_setprio(0);
#pragma unroll 1
        for (int it = 0; it < SPIN_MAX; ++it) {
            if (__builtin_amdgcn_readfirstlane(__hip_atomic_load((LAS u32*)(uintptr_t)(M_CNT + 12), __ATOMIC_RELAXED, __HIP_MEMORY_SCOPE_WORKGROUP)) >= 1) break;
            __builtin_amdgcn_s_sleep(2);
        }
        asm volatile("buffer_load_dwordx4 %0, %1, %2, 0 offen" : "=v"(xres) : "v"((u32)(64 * mt + (tid >> 3)) * 896 + nj * 128 + (tid & 7) * 16), "s"(rX) : "memory");
        ld4c(sres, (u32)(64 * mt + (tid >> 3)) * 8, rWS, WS_ST);
        {
            const u32 v = (u32)(64 * nj + 16 * w4 + (lane >> 4)) * 8192 + 256 * mt + (lane & 15) * 16;
            u32x4 g0, g1, g2, g3;
#pragma unroll 1
            for (int it = 0; it < SPIN_MAX; ++it) {
                ld16c(g0, v, rWS, WS_ZT); ld16c(g1, v, rWS, WS_ZT + 32768); ld16c(g2, v, rWS, WS_ZT + 65536); ld16c(g3, v, rWS, WS_ZT + 98304);
                asm volatile("s_waitcnt vmcnt(0)" : "+v"(g0), "+v"(g1), "+v"(g2), "+v"(g3), "+v"(xres), "+v"(sres) :: "memory");
                const u32 bad = (g0[1] ^ tag.x) | (g0[3] ^ tag.x) | (g1[1] ^ tag.x) | (g1[3] ^ tag.x) | (g2[1] ^ tag.x) | (g2[3] ^ tag.x) | (g3[1] ^ tag.x) | (g3[3] ^ tag.x);
                if (__all(bad == 0)) break;
                __builtin_amdgcn_s_sleep(ZSLEEP);
            }

            const u32 zw = ZTL + 2 * w4 * 1040 + (lane >> 4) * 128 + (lane & 15) * 8;
            ldsw<u32x2>(zw, u32x2{g0[0], g0[2]}); ldsw<u32x2>(zw + 512, u32x2{g1[0], g1[2]});
            ldsw<u32x2>(zw + 1040, u32x2{g2[0], g2[2]}); ldsw<u32x2>(zw + 1552, u32x2{g3[0], g3[2]});
            const u32 za = ZTL + 2 * w4 * 1040 + ((lane & 15) >> 2) * 128 + ((lane >> 4) * 16 + (lane & 3) * 4) * 2;
            asm volatile("" ::: "memory");
            const u32x4 z0 = ldstr2(za, za + 512), z1 = ldstr2(za + 1040, za + 1552);
            ldsw<u32x4>(ZT2 + lane * 128 + (((2 * w4) ^ (lane & 7)) << 4), z0);
            ldsw<u32x4>(ZT2 + lane * 128 + (((2 * w4 + 1) ^ (lane & 7)) << 4), z1);
        }

    }
    BAR();
    {
        const int l = tid >> 3, dc = tid & 7;
        const u32 i0 = (l >> 5) * 16384 + (l & 31) * 128 + ((dc ^ (l & 7)) << 4);
        const u32x4 p0 = ldsr<u32x4>(i0), p1 = ldsr<u32x4>(i0 + 4096), p2 = ldsr<u32x4>(i0 + 8192), p3 = ldsr<u32x4>(i0 + 12288);
        const float rs = __builtin_amdgcn_rcpf(bfhi(sres)), rs2 = rs * rs;
        const u32x4 zq = ldsr<u32x4>(ZT2 + l * 128 + ((dc ^ (l & 7)) << 4));
        float o[8];
#pragma unroll
        for (int e = 0; e < 8; ++e) {
            const float z = (e & 1) ? bfhi(zq[e >> 1]) : bflo(zq[e >> 1]);
            const float xr = (e & 1) ? bfhi(xres[e >> 1]) : bflo(xres[e >> 1]);
            const int j = e >> 1;
            const float f = (e & 1) ? (bfhi(p0[j]) + bfhi(p1[j])) + (bfhi(p2[j]) + bfhi(p3[j])) : (bflo(p0[j]) + bflo(p1[j])) + (bflo(p2[j]) + bflo(p3[j]));
            o[e] = __builtin_fmaf(f, rs2, xr + z);
        }
        *(u32x4*)(P.out + (size_t)(64 * mt + l) * 896 + nj * 128 + dc * 16) = u32x4{pkbf(o[0], o[1]), pkbf(o[2], o[3]), pkbf(o[4], o[5]), pkbf(o[6], o[7])};
    }

}

extern "C" void kernel_launch(void* const* d_in, const int* in_sizes, int n_in, void* d_out, int out_size, void* d_ws, size_t ws_size,
                              hipStream_t stream) {
    Params P;
    P.x = (const char*)d_in[0]; P.logA = (const char*)d_in[1]; P.Aim = (const char*)d_in[2]; P.Cr = (const char*)d_in[3];
    P.Ci = (const char*)d_in[4]; P.logdt = (const char*)d_in[5]; P.Dsk = (const char*)d_in[6];
    P.Wg = (const char*)d_in[8]; P.Wu = (const char*)d_in[9]; P.Wd = (const char*)d_in[10];
    P.out = (char*)d_out; P.ws = (char*)d_ws;
    (void)hipFuncSetAttribute((const void*)fused_kernel, hipFuncAttributeMaxDynamicSharedMemorySize, LDS_BYTES);
    hipLaunchKernelGGL(fused_kernel, dim3(256), dim3(512), LDS_BYTES, stream, P);
}
```
